```python
import torch

def _softmax(x, axis=-1):
    return torch.softmax(x, dim=axis)
def _gelu(x):
    return torch.nn.functional.gelu(x, approximate="tanh")
def _silu(x):
    return torch.nn.functional.silu(x)
def _sigmoid(x):
    return torch.sigmoid(x)
def _layernorm(x, axis=-1, eps=1e-5):
    m = x.mean(dim=axis, keepdim=True)
    v = x.var(dim=axis, keepdim=True, unbiased=False)
    return (x - m) / torch.sqrt(v + eps)
def _scatter_add(tgt, idx, src, axis=0):
    return tgt.clone().index_add_(axis, idx, src)
def _segment_sum(src, seg, n):
    out = torch.zeros((n,) + src.shape[1:], dtype=src.dtype)
    return out.index_add_(0, seg, src)
def _pad(x, pw):
    flat = tuple(v for pair in reversed(pw) for v in pair)
    return torch.nn.functional.pad(x, flat)

kernel_name = 'sequential_preLN__ssm_conv_s4d__glu_gated_swiglu__prompt'

def setup_inputs():

    _w = (lambda a: (lambda t: t.to(torch.bfloat16) if t.is_floating_point() else t)(torch.as_tensor(a)))
    def _A_setup():
        Bsz, L, D, N = 1, 2048, 448, 256
        return {
            "u": _w((lambda *s: torch.randn(*s, dtype=torch.bfloat16) * 0.05)(Bsz, L, D)),
            "log_A_real": _w((lambda *s: torch.randn(*s, dtype=torch.bfloat16) * 6.4)(D, N) * 0.5),
            "A_imag": _w((lambda *s: torch.randn(*s, dtype=torch.bfloat16) * 0.05)(D, N) * 3.14),
            "C_real": _w((lambda *s: torch.randn(*s, dtype=torch.bfloat16) * 0.05)(D, N) / N ** 0.5),
            "C_imag": _w((lambda *s: torch.randn(*s, dtype=torch.bfloat16) * 0.05)(D, N) / N ** 0.5),
            "log_dt": _w((lambda *s: torch.randn(*s, dtype=torch.bfloat16) * 0.05)(D) * 0.5 - 4.0),
            "D_skip": _w((lambda *s: torch.randn(*s, dtype=torch.bfloat16) * 0.05)(D)),
        }
    _A_in = _A_setup()

    def _B_setup():
        B, S, D, F = 1, 2048, 448, 1792
        return {
            "x": _w((lambda *s: torch.randn(*s, dtype=torch.bfloat16) * 0.05)(B, S, D)),
            "w1": _w((lambda *s: torch.randn(*s, dtype=torch.bfloat16) * 0.4)(D, F) * (2.0 / D) ** 0.5),
            "w3": _w((lambda *s: torch.randn(*s, dtype=torch.bfloat16) * 0.4)(D, F) * (2.0 / D) ** 0.5),
            "w2": _w((lambda *s: torch.randn(*s, dtype=torch.bfloat16) * 0.4)(F, D) * (2.0 / F) ** 0.5),
        }
    _B_in = _B_setup()

    return {
        "A_u": _A_in["u"],
        "A_log_A_real": _A_in["log_A_real"],
        "A_A_imag": _A_in["A_imag"],
        "A_C_real": _A_in["C_real"],
        "A_C_imag": _A_in["C_imag"],
        "A_log_dt": _A_in["log_dt"],
        "A_D_skip": _A_in["D_skip"],
        "B_x": _B_in["x"],
        "B_w1": _B_in["w1"],
        "B_w3": _B_in["w3"],
        "B_w2": _B_in["w2"],
    }

def reference(A_u, A_log_A_real, A_A_imag, A_C_real, A_C_imag, A_log_dt, A_D_skip, B_x, B_w1, B_w3, B_w2):
    def _A_ref(u, log_A_real, A_imag, C_real, C_imag, log_dt, D_skip):
        L = u.shape[1]
        dt = torch.exp(log_dt)
        A = -torch.exp(log_A_real) + 1j * A_imag
        C = (C_real + 1j * C_imag) * dt[:, None]
        pos = (lambda x, dt: x.to(dt))(torch.arange(L), log_dt.dtype)
        P = torch.exp((A * dt[:, None])[:, :, None] * pos[None, None, :])
        K = 2.0 * torch.einsum("dn,dnl->dl", C, P).real
        L2 = 2 * L
        u_t = (lambda x, *d: x.permute(*d))(u, 0, 2, 1)
        Kf = (lambda x, *a, **k: torch.fft.rfft(x.float(), *a, **k))(K, L2)
        Uf = (lambda x, *a, **k: torch.fft.rfft(x.float(), *a, **k))(u_t, L2)
        y = (lambda x, *a, **k: torch.fft.irfft(x, *a, **k).to(torch.bfloat16))(Uf * Kf[None, :, :], L2)[:, :, :L]
        y = (lambda x, *d: x.permute(*d))(y, 0, 2, 1)
        return y + u * D_skip[None, None, :]

    def _B_ref(x, w1, w3, w2):
        gate = torch.einsum("bsd,df->bsf", x, w1)
        up = torch.einsum("bsd,df->bsf", x, w3)
        h = _silu(gate) * up
        out = torch.einsum("bsf,fd->bsd", h, w2)
        return out

    x = A_u
    h = x + _A_ref(_layernorm(x, axis=-1), A_log_A_real, A_A_imag, A_C_real, A_C_imag, A_log_dt, A_D_skip)
    h2 = h + _B_ref(_layernorm(h, axis=-1), B_w1, B_w3, B_w2)
    return h2
```

```cpp
#include <hip/hip_runtime.h>
#include <hip/hip_bf16.h>
#include <stdint.h>
typedef unsigned short u16;
#define L_ 2048
#define D_ 448
#define N_ 256
#define F_ 1792
__device__ __forceinline__ float b2f(u16 v){ return __uint_as_float(((unsigned)v)<<16); }
__device__ __forceinline__ u16 f2b(float f){ unsigned u=__float_as_uint(f); u += 0x7fff + ((u>>16)&1); return (u16)(u>>16); }
__device__ __forceinline__ float rb(float f){ return b2f(f2b(f)); }

__global__ void k_ln(const u16* x, u16* u){
  int row = blockIdx.x*4 + threadIdx.x/64, lane = threadIdx.x&63;
  float v[7]; float s=0;
  for(int i=0;i<7;i++){ v[i]=b2f(x[row*D_+lane+64*i]); s+=v[i]; }
  for(int o=32;o;o>>=1) s+=__shfl_xor(s,o);
  float m=s/D_; float q=0;
  for(int i=0;i<7;i++){ float d=v[i]-m; q+=d*d; }
  for(int o=32;o;o>>=1) q+=__shfl_xor(q,o);
  float mb=rb(m), vb=rb(q/D_); float sd=rb(sqrtf(rb(vb+1e-5f)));
  for(int i=0;i<7;i++) u[row*D_+lane+64*i]=f2b(rb(v[i]-mb)/sd);
}
__global__ void k_kgen(const u16* lA,const u16* Ai,const u16* Cr,const u16* Ci,const u16* ldt,float* K){
  int d=blockIdx.x; __shared__ float zr[N_],zi[N_],cr[N_],ci[N_];
  float dt=rb(expf(b2f(ldt[d])));
  int n=threadIdx.x; zr[n]=-rb(expf(b2f(lA[d*N_+n])))*dt; zi[n]=b2f(Ai[d*N_+n])*dt; cr[n]=b2f(Cr[d*N_+n])*dt; ci[n]=b2f(Ci[d*N_+n])*dt;
  __syncthreads();
  for(int l=threadIdx.x;l<L_;l+=256){ float p=rb((float)l); float s=0;
    for(int k=0;k<N_;k++){ float e=__expf(zr[k]*p); float a=zi[k]*p; s+=e*(cr[k]*__cosf(a)-ci[k]*__sinf(a)); }
    K[d*L_+l]=2.f*s; }
}
__global__ void k_conv(const u16* x,const u16* u,const float* K,const u16* Ds,u16* h){
  int d=blockIdx.x; __shared__ float ks[L_],us[L_];
  for(int l=threadIdx.x;l<L_;l+=256){ ks[l]=K[d*L_+l]; us[l]=b2f(u[l*D_+d]); }
  __syncthreads();
  float ds=b2f(Ds[d]);
  for(int t=threadIdx.x;t<L_;t+=256){ float s=0; for(int l=0;l<=t;l++) s+=ks[l]*us[t-l];
    float y=rb(s); float a=rb(y+rb(us[t]*ds)); h[t*D_+d]=f2b(b2f(x[t*D_+d])+a); }
}
__global__ void k_g1(const u16* xn,const u16* w1,const u16* w3,u16* hm){
  int t=blockIdx.x; __shared__ float xs[D_];
  for(int i=threadIdx.x;i<D_;i+=256) xs[i]=b2f(xn[t*D_+i]); __syncthreads();
  for(int f=threadIdx.x;f<F_;f+=256){ float g=0,u=0; for(int k=0;k<D_;k++){ g+=xs[k]*b2f(w1[k*F_+f]); u+=xs[k]*b2f(w3[k*F_+f]); }
    g=rb(g);u=rb(u); float s=rb(g/(1.f+__expf(-g))); hm[t*F_+f]=f2b(s*u); }
}
__global__ void k_g2(const u16* hm,const u16* w2,const u16* h,u16* out){
  int t=blockIdx.x; __shared__ float xs[F_];
  for(int i=threadIdx.x;i<F_;i+=256) xs[i]=b2f(hm[t*F_+i]); __syncthreads();
  for(int d=threadIdx.x;d<D_;d+=256){ float s=0; for(int k=0;k<F_;k++) s+=xs[k]*b2f(w2[k*D_+d]);
    out[t*D_+d]=f2b(b2f(h[t*D_+d])+rb(s)); }
}
extern "C" void kernel_launch(void* const* d_in, const int* in_sizes, int n_in, void* d_out, int out_size, void* d_ws, size_t ws_size, hipStream_t stream){
  const u16* x=(const u16*)d_in[0];
  char* ws=(char*)d_ws;
  u16* u=(u16*)ws; float* K=(float*)(ws+(4<<20)); u16* h=(u16*)(ws+(8<<20)); u16* xn=(u16*)(ws+(12<<20)); u16* hm=(u16*)(ws+(16<<20));
  k_ln<<<L_/4,256,0,stream>>>(x,u);
  k_kgen<<<D_,256,0,stream>>>((const u16*)d_in[1],(const u16*)d_in[2],(const u16*)d_in[3],(const u16*)d_in[4],(const u16*)d_in[5],K);
  k_conv<<<D_,256,0,stream>>>(x,u,K,(const u16*)d_in[6],h);
  k_ln<<<L_/4,256,0,stream>>>(h,xn);
  k_g1<<<L_,256,0,stream>>>(xn,(const u16*)d_in[8],(const u16*)d_in[9],hm);
  k_g2<<<L_,256,0,stream>>>(hm,(const u16*)d_in[10],h,(u16*)d_out);
}
```

```cpp
#include <hip/hip_runtime.h>
#include <hip/hip_bf16.h>

struct Params { const void *x, *lA, *Ai, *Cr, *Ci, *ldt, *Dk, *w1, *w3, *w2; void *out, *ws; };

__global__ __launch_bounds__(256, 1) void s4d_ffn_kernel(Params P) {
  __shared__ __attribute__((aligned(1024))) char smem[163840];
  asm volatile(
  "s_mov_b64 s[96:97], %0\n"
  "s_mov_b32 s98, %1\n"
  "v_mov_b32 v1, %2\n"
  "s_load_dwordx16 s[36:51], s[96:97], 0x0\n"
  "s_load_dwordx8 s[52:59], s[96:97], 0x40\n"
  "s_getpc_b64 s[60:61]\n"
  "LPC_%=:\n"
  "s_lshr_b32 s4, s98, 3\n"
  "s_lshl_b32 s4, s4, 11\n"
  "v_lshrrev_b32 v10, 4, v1\n"
  "v_lshl_add_u32 v10, v10, 7, s4\n"
  "v_min_u32 v10, LEND_%=-LPC_%=-4, v10\n"
  "global_load_dword v12, v10, s[60:61]\n"
  "v_readfirstlane_b32 s3, v1\n"
  "v_and_b32 v2, 63, v1\n"
  "s_lshr_b32 s3, s3, 6\n"
  "s_mul_i32 s4, s98, 7168\n"
  "v_and_b32 v8, 3, v1\n"
  "v_lshrrev_b32 v9, 2, v2\n"
  "s_mul_i32 s5, s3, 224\n"
  "s_add_u32 s5, s5, s4\n"
  "v_mul_u32_u24 v6, 1792, v8\n"
  "v_lshl_add_u32 v6, v9, 4, v6\n"
  "v_add_u32 v6, s5, v6\n"
  "s_mul_i32 s5, s3, 1792\n"
  "s_add_u32 s5, s5, s4\n"
  "v_lshl_add_u32 v5, v2, 4, s5\n"
  "v_cmp_gt_u32 vcc, 56, v2\n"
  "v_mov_b32 v10, 2146435072\n"
  "s_nop 0\n"
  "v_cndmask_b32 v6, v10, v6, vcc\n"
  "v_cndmask_b32 v5, v10, v5, vcc\n"
  "s_and_b32 s12, s98, 7\n"
  "s_lshr_b32 s13, s98, 3\n"
  "s_mul_i32 s9, s12, 28\n"
  "s_cmp_lt_u32 s13, 28\n"
  "s_cselect_b32 s6, s13, 0\n"
  "s_add_u32 s9, s9, s6\n"
  "s_lshr_b32 s14, s3, 1\n"
  "s_and_b32 s15, s3, 1\n"
  "s_mov_b32 s22, 1835008\n"
  "s_mov_b32 s23, 0x20000\n"
  "s_waitcnt lgkmcnt(0)\n"
  "s_mov_b32 s20, s36\n"
  "s_mov_b32 s21, s37\n"
  "s_load_dword s8, s[58:59], 0x0\n"
  "s_lshl_b32 s4, s9, 2\n"
  "s_load_dword s11, s[46:47], s4\n"
  "s_load_dword s10, s[48:49], s4\n"
  "s_lshl_b32 s4, s9, 10\n"
  "s_lshl_b32 s5, s14, 9\n"
  "s_add_u32 s4, s4, s5\n"
  "v_lshl_add_u32 v10, v2, 3, s4\n"
  "s_lshl_b32 s5, s15, 2\n"
  "v_add_u32 v11, s5, v10\n"
  "global_load_dword v13, v11, s[38:39] nt\n"
  "global_load_dword v18, v11, s[40:41] nt\n"
  "global_load_dwordx2 v[14:15], v10, s[42:43] nt\n"
  "global_load_dwordx2 v[16:17], v10, s[44:45] nt\n"
  "buffer_load_dwordx4 v[20:23], v6, s[20:23], 0 offen nt\n"
  "buffer_load_dwordx4 v[24:27], v6, s[20:23], 0 offen offset:896 nt\n"
  "buffer_load_dwordx4 v[28:31], v5, s[20:23], 0 offen nt\n"
  "buffer_load_dwordx4 v[32:35], v5, s[20:23], 0 offen offset:896 nt\n"
  "s_mov_b32 s24, s58\n"
  "s_mov_b32 s25, s59\n"
  "s_mov_b32 s26, 33554432\n"
  "s_mov_b32 s27, 0x20000\n"
  "s_mov_b32 s28, s50\n"
  "s_mov_b32 s29, s51\n"
  "s_mov_b32 s30, 1605632\n"
  "s_mov_b32 s31, 0x20000\n"
  "s_mov_b32 s32, s52\n"
  "s_mov_b32 s33, s53\n"
  "s_mov_b32 s34, 1605632\n"
  "s_mov_b32 s35, 0x20000\n"
  "s_mul_i32 s4, s3, 917504\n"
  "s_lshl_b32 s5, s98, 6\n"
  "s_add_u32 s4, s4, s5\n"
  "s_add_u32 s4, s4, 1048576\n"
  "v_lshlrev_b32 v7, 16, v9\n"
  "v_lshl_add_u32 v7, v8, 4, v7\n"
  "v_add_u32 v7, s4, v7\n"
  "v_mov_b32 v10, 2146435072\n"
  "v_cndmask_b32 v7, v10, v7, vcc\n"
  "v_and_b32 v3, 31, v1\n"
  "v_lshrrev_b32 v4, 5, v2\n"
  "s_mov_b32 s19, 400000\n"
  "s_lshl_b32 s4, s98, 2\n"
  "s_add_u32 s4, s4, s3\n"
  "s_mov_b32 s62, s4\n"
  "s_cmp_eq_u32 s4, 1020\n"
  "s_cbranch_scc1 L1_%=\n"
  "L2_%=:\n"
  "s_mul_i32 s36, s3, 17408\n"
  "s_lshl_b32 s37, s3, 10\n"
  "s_lshl_b32 s38, s3, 12\n"
  "v_lshl_add_u32 v10, v4, 4, s36\n"
  "v_mul_u32_u24 v19, 272, v3\n"
  "v_add_u32 v19, v19, v10\n"
  "v_and_b32 v36, 15, v2\n"
  "v_lshrrev_b32 v37, 4, v2\n"
  "s_lshl_b32 s4, s9, 14\n"
  "s_add_u32 s4, s4, 1048576\n"
  "v_lshl_add_u32 v38, v1, 4, s4\n"
  "v_mov_b32 v39, 131072\n"
  "v_lshl_add_u32 v39, v1, 4, v39\n"
  "v_lshrrev_b32 v40, 4, v1\n"
  "v_mul_u32_u24 v40, 96, v40\n"
  "v_xor_b32 v11, 3, v36\n"
  "v_lshl_add_u32 v40, v11, 2, v40\n"
  "v_add_u32 v10, 1, v36\n"
  "v_and_b32 v10, 3, v10\n"
  "v_mul_u32_u24 v41, 4158, v10\n"
  "v_lshl_add_u32 v41, v36, 1, v41\n"
  "v_lshlrev_b32 v11, 4, v37\n"
  "v_sub_u32 v41, v41, v11\n"
  "s_mul_i32 s4, s14, 16640\n"
  "s_lshl_b32 s5, s15, 9\n"
  "s_add_u32 s4, s4, s5\n"
  "s_add_u32 s4, s4, 50\n"
  "v_add_u32 v41, s4, v41\n"
  "v_mul_u32_u24 v42, 96, v36\n"
  "v_add_u32 v42, v42, v11\n"
  "s_mul_i32 s4, s14, 7584\n"
  "s_mul_i32 s5, s15, 768\n"
  "s_sub_u32 s4, s4, s5\n"
  "s_add_u32 s4, s4, 34048\n"
  "v_add_u32 v42, s4, v42\n"
  "s_lshr_b32 s4, s12, 1\n"
  "s_lshl_b32 s4, s4, 2\n"
  "s_and_b32 s5, s13, 3\n"
  "s_add_u32 s16, s4, s5\n"
  "s_and_b32 s4, s12, 1\n"
  "s_mul_i32 s4, s4, 7\n"
  "s_lshr_b32 s5, s13, 2\n"
  "s_add_u32 s17, s4, s5\n"
  "s_and_b32 s18, s16, 1\n"
  "s_mul_i32 s40, s18, 802816\n"
  "s_sub_u32 s41, 802816, s40\n"
  "v_and_b32 v10, 1, v37\n"
  "s_lshl_b32 s4, s15, 1\n"
  "v_or_b32 v10, s4, v10\n"
  "v_lshlrev_b32 v10, 2, v10\n"
  "v_xor_b32 v10, v10, v36\n"
  "v_mul_u32_u24 v43, 3584, v37\n"
  "v_lshl_add_u32 v43, v10, 4, v43\n"
  "s_mul_i32 s4, s3, 14336\n"
  "s_lshl_b32 s5, s17, 8\n"
  "s_add_u32 s4, s4, s5\n"
  "v_add_u32 v43, s4, v43\n"
  "s_lshl_b32 s4, s12, 11\n"
  "s_add_u32 s4, s4, 4096\n"
  "v_lshl_add_u32 v44, v2, 8, s4\n"
  "s_lshl_b32 s4, s12, 8\n"
  "s_add_u32 s4, s4, 4096\n"
  "v_and_b32 v10, 7, v2\n"
  "v_lshl_add_u32 v45, v10, 11, s4\n"
  "v_mov_b32 v48, 1\n"
  "v_bfe_u32 v10, v1, 3, 3\n"
  "v_mul_u32_u24 v46, 320, v10\n"
  "v_and_b32 v10, 7, v1\n"
  "v_lshl_add_u32 v46, v10, 3, v46\n"
  "s_lshl_b32 s4, s3, 6\n"
  "s_add_u32 s4, s4, 28672\n"
  "v_add_u32 v46, s4, v46\n"
  "s_lshl_b32 s4, s9, 13\n"
  "s_add_u32 s4, s4, 5242880\n"
  "v_lshl_add_u32 v47, v1, 3, s4\n"
  "v_and_b32 v10, 1, v37\n"
  "v_mul_u32_u24 v49, 57344, v10\n"
  "v_lshl_add_u32 v49, v4, 8, v49\n"
  "v_lshl_add_u32 v49, v36, 4, v49\n"
  "s_mul_i32 s42, s16, 458752\n"
  "s_lshl_b32 s4, s17, 2\n"
  "s_add_u32 s4, s4, s3\n"
  "s_lshl_b32 s5, s4, 10\n"
  "s_add_u32 s42, s42, s5\n"
  "s_add_u32 s42, s42, 7340032\n"
  "s_waitcnt lgkmcnt(0)\n"
  "s_add_u32 s8, s8, 1\n"
  "v_mov_b32 v51, s8\n"
  "v_mov_b32 v53, s8\n"
  "v_mov_b32 v55, s8\n"
  "v_mov_b32 v57, s8\n"
  "s_waitcnt vmcnt(2)\n"
  "v_mov_b32 v50, v20\n"
  "v_mov_b32 v52, v24\n"
  "buffer_store_dwordx4 v[50:53], v7, s[24:27], 0 offen sc1\n"
  "v_add_u32 v7, 0x4000, v7\n"
  "v_mov_b32 v54, v21\n"
  "v_mov_b32 v56, v25\n"
  "buffer_store_dwordx4 v[54:57], v7, s[24:27], 0 offen sc1\n"
  "v_add_u32 v7, 0x4000, v7\n"
  "v_mov_b32 v50, v22\n"
  "v_mov_b32 v52, v26\n"
  "buffer_store_dwordx4 v[50:53], v7, s[24:27], 0 offen sc1\n"
  "v_add_u32 v7, 0x4000, v7\n"
  "v_mov_b32 v54, v23\n"
  "v_mov_b32 v56, v27\n"
  "buffer_store_dwordx4 v[54:57], v7, s[24:27], 0 offen sc1\n"
  "s_mov_b32 s4, 0x3f803f80\n"
  "s_waitcnt vmcnt(4)\n"
  "v_dot2_f32_bf16 v58, v28, s4, 0\n"
  "v_dot2_f32_bf16 v59, v28, v28, 0\n"
  "v_dot2_f32_bf16 v60, v32, s4, 0\n"
  "v_dot2_f32_bf16 v61, v32, v32, 0\n"
  "v_dot2_f32_bf16 v58, v29, s4, v58\n"
  "v_dot2_f32_bf16 v59, v29, v29, v59\n"
  "v_dot2_f32_bf16 v60, v33, s4, v60\n"
  "v_dot2_f32_bf16 v61, v33, v33, v61\n"
  "v_dot2_f32_bf16 v58, v30, s4, v58\n"
  "v_dot2_f32_bf16 v59, v30, v30, v59\n"
  "v_dot2_f32_bf16 v60, v34, s4, v60\n"
  "v_dot2_f32_bf16 v61, v34, v34, v61\n"
  "v_dot2_f32_bf16 v58, v31, s4, v58\n"
  "v_dot2_f32_bf16 v59, v31, v31, v59\n"
  "v_dot2_f32_bf16 v60, v35, s4, v60\n"
  "v_dot2_f32_bf16 v61, v35, v35, v61\n"
  "v_add_f32_dpp v58, v58, v58 row_shr:1 row_mask:0xf bank_mask:0xf bound_ctrl:1\n"
  "v_add_f32_dpp v59, v59, v59 row_shr:1 row_mask:0xf bank_mask:0xf bound_ctrl:1\n"
  "v_add_f32_dpp v60, v60, v60 row_shr:1 row_mask:0xf bank_mask:0xf bound_ctrl:1\n"
  "v_add_f32_dpp v61, v61, v61 row_shr:1 row_mask:0xf bank_mask:0xf bound_ctrl:1\n"
  "v_add_f32_dpp v58, v58, v58 row_shr:2 row_mask:0xf bank_mask:0xf bound_ctrl:1\n"
  "v_add_f32_dpp v59, v59, v59 row_shr:2 row_mask:0xf bank_mask:0xf bound_ctrl:1\n"
  "v_add_f32_dpp v60, v60, v60 row_shr:2 row_mask:0xf bank_mask:0xf bound_ctrl:1\n"
  "v_add_f32_dpp v61, v61, v61 row_shr:2 row_mask:0xf bank_mask:0xf bound_ctrl:1\n"
  "v_add_f32_dpp v58, v58, v58 row_shr:4 row_mask:0xf bank_mask:0xf bound_ctrl:1\n"
  "v_add_f32_dpp v59, v59, v59 row_shr:4 row_mask:0xf bank_mask:0xf bound_ctrl:1\n"
  "v_add_f32_dpp v60, v60, v60 row_shr:4 row_mask:0xf bank_mask:0xf bound_ctrl:1\n"
  "v_add_f32_dpp v61, v61, v61 row_shr:4 row_mask:0xf bank_mask:0xf bound_ctrl:1\n"
  "v_add_f32_dpp v58, v58, v58 row_shr:8 row_mask:0xf bank_mask:0xf bound_ctrl:1\n"
  "v_add_f32_dpp v59, v59, v59 row_shr:8 row_mask:0xf bank_mask:0xf bound_ctrl:1\n"
  "v_add_f32_dpp v60, v60, v60 row_shr:8 row_mask:0xf bank_mask:0xf bound_ctrl:1\n"
  "v_add_f32_dpp v61, v61, v61 row_shr:8 row_mask:0xf bank_mask:0xf bound_ctrl:1\n"
  "v_add_f32_dpp v58, v58, v58 row_bcast:15 row_mask:0xf bank_mask:0xf bound_ctrl:1\n"
  "v_add_f32_dpp v59, v59, v59 row_bcast:15 row_mask:0xf bank_mask:0xf bound_ctrl:1\n"
  "v_add_f32_dpp v60, v60, v60 row_bcast:15 row_mask:0xf bank_mask:0xf bound_ctrl:1\n"
  "v_add_f32_dpp v61, v61, v61 row_bcast:15 row_mask:0xf bank_mask:0xf bound_ctrl:1\n"
  "v_add_f32_dpp v58, v58, v58 row_bcast:31 row_mask:0xf bank_mask:0xf bound_ctrl:1\n"
  "v_add_f32_dpp v59, v59, v59 row_bcast:31 row_mask:0xf bank_mask:0xf bound_ctrl:1\n"
  "v_add_f32_dpp v60, v60, v60 row_bcast:31 row_mask:0xf bank_mask:0xf bound_ctrl:1\n"
  "v_add_f32_dpp v61, v61, v61 row_bcast:31 row_mask:0xf bank_mask:0xf bound_ctrl:1\n"
  "s_mov_b32 s4, 0x3b124925\n"
  "v_mul_f32 v58, s4, v58\n"
  "v_mul_f32 v59, s4, v59\n"
  "v_mul_f32 v60, s4, v60\n"
  "v_mul_f32 v61, s4, v61\n"
  "v_fma_f32 v59, -v58, v58, v59\n"
  "v_fma_f32 v61, -v60, v60, v61\n"
  "v_max_f32 v59, 0, v59\n"
  "v_max_f32 v61, 0, v61\n"
  "v_cvt_pk_bf16_f32 v10, v59, v61\n"
  "v_lshlrev_b32 v59, 16, v10\n"
  "v_and_b32 v61, 0xffff0000, v10\n"
  "v_add_f32 v59, 0x3727c5ac, v59\n"
  "v_add_f32 v61, 0x3727c5ac, v61\n"
  "v_cvt_pk_bf16_f32 v10, v59, v61\n"
  "v_lshlrev_b32 v59, 16, v10\n"
  "v_and_b32 v61, 0xffff0000, v10\n"
  "v_sqrt_f32 v59, v59\n"
  "v_sqrt_f32 v61, v61\n"
  "s_lshl_b32 s4, s3, 3\n"
  "v_mov_b32 v10, s4\n"
  "v_cvt_pk_bf16_f32 v20, v58, v59\n"
  "v_cvt_pk_bf16_f32 v21, v60, v61\n"
  "s_mov_b32 exec_lo, 0\n"
  "s_brev_b32 exec_hi, 1\n"
  "ds_write_b64 v10, v[20:21] offset:16048\n"
  "s_mov_b64 exec, -1\n"
  "s_cmp_eq_u32 s3, 0\n"
  "s_cselect_b64 s[44:45], -1, 0\n"
  "s_cmp_ge_u32 s13, 28\n"
  "s_cbranch_scc1 L3_%=\n"
  "s_mov_b32 s20, 0x3fb8aa3b\n"
  "s_lshl_b32 s4, s11, 16\n"
  "s_and_b32 s5, s11, 0xffff0000\n"
  "s_cmp_eq_u32 s14, 0\n"
  "s_cselect_b32 s4, s4, s5\n"
  "v_mov_b32 v5, s4\n"
  "s_waitcnt vmcnt(8)\n"
  "s_mov_b32 s4, 0x7fff7fff\n"
  "v_and_b32 v6, s4, v14\n"
  "v_and_b32 v10, s4, v15\n"
  "v_pk_max_u16 v6, v6, v10\n"
  "v_and_b32 v11, s4, v16\n"
  "v_and_b32 v10, s4, v17\n"
  "v_pk_max_u16 v11, v11, v10\n"
  "v_pk_max_u16 v6, v6, v11\n"
  "v_lshrrev_b32 v10, 16, v6\n"
  "v_and_b32 v6, 0xffff, v6\n"
  "v_max_u32 v6, v6, v10\n"
  "v_lshlrev_b32 v7, 16, v13\n"
  "v_and_b32 v58, 0xffff0000, v13\n"
  "v_mul_f32 v63, s20, v5\n"
  "v_max_u32_dpp v6, v6, v6 row_shr:1 row_mask:0xf bank_mask:0xf bound_ctrl:1\n"
  "v_mul_f32 v61, s20, v7\n"
  "v_mul_f32 v62, s20, v58\n"
  "v_fma_f32 v66, v5, s20, -v63\n"
  "v_max_u32_dpp v6, v6, v6 row_shr:2 row_mask:0xf bank_mask:0xf bound_ctrl:1\n"
  "v_fma_f32 v64, v7, s20, -v61\n"
  "v_fma_f32 v65, v58, s20, -v62\n"
  "v_fmac_f32 v66, 0x32a57060, v5\n"
  "v_max_u32_dpp v6, v6, v6 row_shr:4 row_mask:0xf bank_mask:0xf bound_ctrl:1\n"
  "v_fmac_f32 v64, 0x32a57060, v7\n"
  "v_fmac_f32 v65, 0x32a57060, v58\n"
  "v_exp_f32 v63, v63\n"
  "v_max_u32_dpp v6, v6, v6 row_shr:8 row_mask:0xf bank_mask:0xf bound_ctrl:1\n"
  "v_exp_f32 v61, v61\n"
  "v_exp_f32 v62, v62\n"
  "v_mul_f32 v66, 0x3f317218, v66\n"
  "v_max_u32_dpp v6, v6, v6 row_bcast:15 row_mask:0xf bank_mask:0xf bound_ctrl:1\n"
  "v_mul_f32 v64, 0x3f317218, v64\n"
  "v_mul_f32 v65, 0x3f317218, v65\n"
  "v_fma_f32 v5, v63, v66, v63\n"
  "v_max_u32_dpp v6, v6, v6 row_bcast:31 row_mask:0xf bank_mask:0xf bound_ctrl:1\n"
  "v_fma_f32 v7, v61, v64, v61\n"
  "v_fma_f32 v58, v62, v65, v62\n"
  "v_readlane_b32 s4, v6, 63\n"
  "v_cvt_pk_bf16_f32 v5, v5, v5\n"
  "v_cvt_pk_bf16_f32 v10, v7, v58\n"
  "v_lshlrev_b32 v5, 16, v5\n"
  "v_lshlrev_b32 v7, 16, v10\n"
  "v_and_b32 v58, 0xffff0000, v10\n"
  "s_lshr_b32 s4, s4, 7\n"
  "s_max_u32 s4, s4, 1\n"
  "s_min_u32 s4, s4, 252\n"
  "s_sub_u32 s21, 253, s4\n"
  "s_lshl_b32 s21, s21, 23\n"
  "s_add_u32 s4, s4, 1\n"
  "s_lshl_b32 s4, s4, 23\n"
  "v_mul_f32 v10, 0xbfb8aa3b, v5\n"
  "v_mul_f32 v11, 0.15915494, v5\n"
  "v_lshlrev_b32 v59, 16, v18\n"
  "v_and_b32 v60, 0xffff0000, v18\n"
  "v_mul_f32 v7, v7, v10\n"
  "v_mul_f32 v59, v59, v11\n"
  "v_mul_f32 v58, v58, v10\n"
  "v_mul_f32 v60, v60, v11\n"
  "v_add_f32 v5, v5, v5\n"
  "v_mul_f32 v5, s4, v5\n"
  "s_cmp_eq_u32 s15, 0\n"
  "s_cselect_b64 vcc, -1, 0\n"
  "v_cndmask_b32 v10, v15, v14, vcc\n"
  "v_cndmask_b32 v11, v17, v16, vcc\n"
  "v_lshlrev_b32 v54, 16, v10\n"
  "v_and_b32 v56, 0xffff0000, v10\n"
  "v_lshlrev_b32 v55, 16, v11\n"
  "v_and_b32 v57, 0xffff0000, v11\n"
  "v_mul_f32 v54, s21, v54\n"
  "v_mul_f32 v55, s21, v55\n"
  "v_mul_f32 v56, s21, v56\n"
  "v_mul_f32 v57, s21, v57\n"
  "v_exp_f32 v61, v7\n"
  "v_fract_f32 v64, v59\n"
  "v_exp_f32 v62, v58\n"
  "v_fract_f32 v65, v60\n"
  "v_cos_f32 v24, v64\n"
  "v_sin_f32 v25, v64\n"
  "v_cos_f32 v26, v65\n"
  "v_sin_f32 v27, v65\n"
  "v_mul_f32 v24, v61, v24\n"
  "v_mul_f32 v25, v61, v25\n"
  "v_mul_f32 v26, v62, v26\n"
  "v_mul_f32 v27, v62, v27\n"
  "v_mul_f32 v61, 0x41000000, v7\n"
  "v_mul_f32 v64, 0x41000000, v59\n"
  "v_exp_f32 v61, v61\n"
  "v_fract_f32 v64, v64\n"
  "v_mul_f32 v62, 0x41000000, v58\n"
  "v_mul_f32 v65, 0x41000000, v60\n"
  "v_exp_f32 v62, v62\n"
  "v_fract_f32 v65, v65\n"
  "v_cos_f32 v28, v64\n"
  "v_sin_f32 v29, v64\n"
  "v_cos_f32 v30, v65\n"
  "v_sin_f32 v31, v65\n"
  "v_mul_f32 v28, v61, v28\n"
  "v_mul_f32 v29, v61, v29\n"
  "v_mul_f32 v30, v62, v30\n"
  "v_mul_f32 v31, v62, v31\n"
  "v_mul_f32 v61, 0x43800000, v7\n"
  "v_mul_f32 v64, 0x43800000, v59\n"
  "v_exp_f32 v61, v61\n"
  "v_fract_f32 v64, v64\n"
  "v_mul_f32 v62, 0x43800000, v58\n"
  "v_mul_f32 v65, 0x43800000, v60\n"
  "v_exp_f32 v62, v62\n"
  "v_fract_f32 v65, v65\n"
  "v_cos_f32 v32, v64\n"
  "v_sin_f32 v33, v64\n"
  "v_cos_f32 v34, v65\n"
  "v_sin_f32 v35, v65\n"
  "v_mul_f32 v32, v61, v32\n"
  "v_mul_f32 v33, v61, v33\n"
  "v_mul_f32 v34, v62, v34\n"
  "v_mul_f32 v35, v62, v35\n"
  "v_lshlrev_b32 v52, 3, v8\n"
  "s_lshl_b32 s4, s98, 6\n"
  "s_add_u32 s4, s4, 131072\n"
  "v_lshl_add_u32 v22, v8, 4, s4\n"
  "s_waitcnt lgkmcnt(0)\n"
  "s_barrier\n"
  "ds_read_b64 v[20:21], v52 offset:16048\n"
  "s_waitcnt lgkmcnt(0)\n"
  "v_mov_b32 v50, v20\n"
  "v_mov_b32 v52, v21\n"
  "s_and_b64 exec, s[44:45], 15\n"
  "buffer_store_dwordx4 v[50:53], v22, s[24:27], 0 offen sc1\n"
  "s_mov_b64 exec, -1\n"
  "v_mov_b32 v6, 0x3c00\n"
  "s_mov_b32 s22, 4096\n"
  "s_mov_b32 s23, 8192\n"
  "s_mov_b32 s43, 12288\n"
  "s_mov_b32 m0, s36\n"
  "v_mov_b32 v168, -1\n"
  "v_mov_b32 v169, -1\n"
  "v_mov_b32 v170, -1\n"
  "v_mov_b32 v171, -1\n"
  "v_pk_mul_f32 v[172:173], v[28:29], v[28:29] op_sel:[0,0] op_sel_hi:[0,1]\n"
  "v_pk_mul_f32 v[174:175], v[24:25], v[24:25] op_sel:[0,0] op_sel_hi:[0,1]\n"
  "v_pk_mul_f32 v[176:177], v[54:55], v[24:25] op_sel:[0,0] op_sel_hi:[0,1]\n"
  "v_pk_mul_f32 v[178:179], v[54:55], v[28:29] op_sel:[0,0] op_sel_hi:[0,1]\n"
  "v_pk_fma_f32 v[180:181], v[28:29], v[28:29], v[172:173] op_sel:[1,1,0] op_sel_hi:[1,0,1] neg_lo:[1,0,0]\n"
  "v_pk_fma_f32 v[188:189], v[24:25], v[24:25], v[174:175] op_sel:[1,1,0] op_sel_hi:[1,0,1] neg_lo:[1,0,0]\n"
  "v_pk_fma_f32 v[192:193], v[54:55], v[24:25], v[176:177] op_sel:[1,1,0] op_sel_hi:[1,0,1] neg_lo:[1,0,0]\n"
  "v_pk_fma_f32 v[206:207], v[54:55], v[28:29], v[178:179] op_sel:[1,1,0] op_sel_hi:[1,0,1] neg_lo:[1,0,0]\n"
  "ds_write_addtid_b32 v6 offset:0\n"
  "v_pk_mul_f32 v[172:173], v[180:181], v[28:29] op_sel:[0,0] op_sel_hi:[0,1]\n"
  "v_cvt_pk_f16_f32 v7, v28, -v29\n"
  "v_pk_mul_f32 v[174:175], v[180:181], v[180:181] op_sel:[0,0] op_sel_hi:[0,1]\n"
  "v_cvt_pk_f16_f32 v13, v54, v55\n"
  "v_pk_mul_f32 v[176:177], v[188:189], v[188:189] op_sel:[0,0] op_sel_hi:[0,1]\n"
  "v_pk_mul_f32 v[178:179], v[54:55], v[188:189] op_sel:[0,0] op_sel_hi:[0,1]\n"
  "v_pk_fma_f32 v[182:183], v[180:181], v[28:29], v[172:173] op_sel:[1,1,0] op_sel_hi:[1,0,1] neg_lo:[1,0,0]\n"
  "v_pk_fma_f32 v[186:187], v[180:181], v[180:181], v[174:175] op_sel:[1,1,0] op_sel_hi:[1,0,1] neg_lo:[1,0,0]\n"
  "v_pk_fma_f32 v[190:191], v[188:189], v[188:189], v[176:177] op_sel:[1,1,0] op_sel_hi:[1,0,1] neg_lo:[1,0,0]\n"
  "v_pk_fma_f32 v[194:195], v[54:55], v[188:189], v[178:179] op_sel:[1,1,0] op_sel_hi:[1,0,1] neg_lo:[1,0,0]\n"
  "v_pk_mul_f32 v[172:173], v[192:193], v[188:189] op_sel:[0,0] op_sel_hi:[0,1]\n"
  "v_cvt_pk_f16_f32 v18, v180, -v181\n"
  "v_pk_mul_f32 v[174:175], v[54:55], v[190:191] op_sel:[0,0] op_sel_hi:[0,1]\n"
  "v_cvt_pk_f16_f32 v58, v192, v193\n"
  "v_pk_mul_f32 v[176:177], v[192:193], v[190:191] op_sel:[0,0] op_sel_hi:[0,1]\n"
  "v_pk_mul_f32 v[178:179], v[194:195], v[190:191] op_sel:[0,0] op_sel_hi:[0,1]\n"
  "v_pk_fma_f32 v[196:197], v[192:193], v[188:189], v[172:173] op_sel:[1,1,0] op_sel_hi:[1,0,1] neg_lo:[1,0,0]\n"
  "v_pk_fma_f32 v[198:199], v[54:55], v[190:191], v[174:175] op_sel:[1,1,0] op_sel_hi:[1,0,1] neg_lo:[1,0,0]\n"
  "v_pk_fma_f32 v[200:201], v[192:193], v[190:191], v[176:177] op_sel:[1,1,0] op_sel_hi:[1,0,1] neg_lo:[1,0,0]\n"
  "v_pk_fma_f32 v[202:203], v[194:195], v[190:191], v[178:179] op_sel:[1,1,0] op_sel_hi:[1,0,1] neg_lo:[1,0,0]\n"
  "v_pk_mul_f32 v[172:173], v[196:197], v[190:191] op_sel:[0,0] op_sel_hi:[0,1]\n"
  "v_cvt_pk_f16_f32 v59, v182, -v183\n"
  "v_pk_mul_f32 v[174:175], v[28:29], v[186:187] op_sel:[0,0] op_sel_hi:[0,1]\n"
  "v_cvt_pk_f16_f32 v96, v186, -v187\n"
  "v_pk_mul_f32 v[176:177], v[180:181], v[186:187] op_sel:[0,0] op_sel_hi:[0,1]\n"
  "ds_write_addtid_b32 v7 offset:272\n"
  "ds_write_addtid_b32 v13 offset:8704\n"
  "ds_write_addtid_b32 v18 offset:544\n"
  "ds_write_addtid_b32 v58 offset:8976\n"
  "ds_write_addtid_b32 v59 offset:816\n"
  "ds_write_addtid_b32 v96 offset:1088\n"
  "s_add_u32 s46, s42, 0\n"
  "buffer_store_dwordx4 v[168:171], v49, s[24:27], s46 offen sc1\n"
  "v_cvt_pk_f16_f32 v97, v194, v195\n"
  "v_pk_mul_f32 v[178:179], v[182:183], v[186:187] op_sel:[0,0] op_sel_hi:[0,1]\n"
  "v_pk_fma_f32 v[204:205], v[196:197], v[190:191], v[172:173] op_sel:[1,1,0] op_sel_hi:[1,0,1] neg_lo:[1,0,0]\n"
  "v_pk_fma_f32 v[28:29], v[28:29], v[186:187], v[174:175] op_sel:[1,1,0] op_sel_hi:[1,0,1] neg_lo:[1,0,0]\n"
  "v_pk_fma_f32 v[180:181], v[180:181], v[186:187], v[176:177] op_sel:[1,1,0] op_sel_hi:[1,0,1] neg_lo:[1,0,0]\n"
  "v_pk_fma_f32 v[182:183], v[182:183], v[186:187], v[178:179] op_sel:[1,1,0] op_sel_hi:[1,0,1] neg_lo:[1,0,0]\n"
  "v_pk_mul_f32 v[172:173], v[186:187], v[186:187] op_sel:[0,0] op_sel_hi:[0,1]\n"
  "v_cvt_pk_f16_f32 v98, v196, v197\n"
  "v_pk_mul_f32 v[174:175], v[54:55], v[32:33] op_sel:[0,0] op_sel_hi:[0,1]\n"
  "v_cvt_pk_f16_f32 v99, v198, v199\n"
  "v_pk_mul_f32 v[176:177], v[194:195], v[32:33] op_sel:[0,0] op_sel_hi:[0,1]\n"
  "v_cvt_pk_f16_f32 v100, v200, v201\n"
  "v_pk_mul_f32 v[178:179], v[198:199], v[32:33] op_sel:[0,0] op_sel_hi:[0,1]\n"
  "v_cvt_pk_f16_f32 v101, v202, v203\n"
  "v_pk_fma_f32 v[184:185], v[186:187], v[186:187], v[172:173] op_sel:[1,1,0] op_sel_hi:[1,0,1] neg_lo:[1,0,0]\n"
  "v_pk_fma_f32 v[54:55], v[54:55], v[32:33], v[174:175] op_sel:[1,1,0] op_sel_hi:[1,0,1] neg_lo:[1,0,0]\n"
  "v_pk_fma_f32 v[194:195], v[194:195], v[32:33], v[176:177] op_sel:[1,1,0] op_sel_hi:[1,0,1] neg_lo:[1,0,0]\n"
  "v_pk_fma_f32 v[198:199], v[198:199], v[32:33], v[178:179] op_sel:[1,1,0] op_sel_hi:[1,0,1] neg_lo:[1,0,0]\n"
  "v_pk_mul_f32 v[172:173], v[202:203], v[32:33] op_sel:[0,0] op_sel_hi:[0,1]\n"
  "v_cvt_pk_f16_f32 v102, v204, v205\n"
  "v_pk_mul_f32 v[174:175], v[206:207], v[32:33] op_sel:[0,0] op_sel_hi:[0,1]\n"
  "ds_write_addtid_b32 v97 offset:9248\n"
  "ds_write_addtid_b32 v98 offset:9520\n"
  "ds_write_addtid_b32 v99 offset:10880\n"
  "ds_write_addtid_b32 v100 offset:11152\n"
  "ds_write_addtid_b32 v101 offset:11424\n"
  "ds_write_addtid_b32 v102 offset:11696\n"
  "buffer_store_dwordx4 v[168:171], v49, s[24:27], s46 offen offset:512 sc1\n"
  "v_cvt_pk_f16_f32 v7, v28, -v29\n"
  "v_pk_mul_f32 v[176:177], v[28:29], v[186:187] op_sel:[0,0] op_sel_hi:[0,1]\n"
  "v_cvt_pk_f16_f32 v13, v180, -v181\n"
  "v_pk_mul_f32 v[178:179], v[180:181], v[186:187] op_sel:[0,0] op_sel_hi:[0,1]\n"
  "v_cvt_pk_f16_f32 v18, v182, -v183\n"
  "v_pk_fma_f32 v[202:203], v[202:203], v[32:33], v[172:173] op_sel:[1,1,0] op_sel_hi:[1,0,1] neg_lo:[1,0,0]\n"
  "v_pk_fma_f32 v[206:207], v[206:207], v[32:33], v[174:175] op_sel:[1,1,0] op_sel_hi:[1,0,1] neg_lo:[1,0,0]\n"
  "v_pk_fma_f32 v[28:29], v[28:29], v[186:187], v[176:177] op_sel:[1,1,0] op_sel_hi:[1,0,1] neg_lo:[1,0,0]\n"
  "v_pk_fma_f32 v[180:181], v[180:181], v[186:187], v[178:179] op_sel:[1,1,0] op_sel_hi:[1,0,1] neg_lo:[1,0,0]\n"
  "v_pk_mul_f32 v[172:173], v[182:183], v[186:187] op_sel:[0,0] op_sel_hi:[0,1]\n"
  "v_cvt_pk_f16_f32 v58, v184, -v185\n"
  "v_pk_mul_f32 v[174:175], v[184:185], v[186:187] op_sel:[0,0] op_sel_hi:[0,1]\n"
  "v_cvt_pk_f16_f32 v59, v54, v55\n"
  "v_pk_mul_f32 v[176:177], v[54:55], v[32:33] op_sel:[0,0] op_sel_hi:[0,1]\n"
  "ds_write_addtid_b32 v7 offset:1360\n"
  "ds_write_addtid_b32 v13 offset:1632\n"
  "ds_write_addtid_b32 v18 offset:1904\n"
  "ds_write_addtid_b32 v58 offset:2176\n"
  "ds_write_addtid_b32 v59 offset:9792\n"
  "ds_write_addtid_b32 v59 offset:10064\n"
  "s_add_u32 s46, s42, 114688\n"
  "buffer_store_dwordx4 v[168:171], v49, s[24:27], s46 offen sc1\n"
  "v_cvt_pk_f16_f32 v96, v194, v195\n"
  "v_pk_mul_f32 v[178:179], v[198:199], v[32:33] op_sel:[0,0] op_sel_hi:[0,1]\n"
  "v_cvt_pk_f16_f32 v97, v198, v199\n"
  "v_pk_fma_f32 v[182:183], v[182:183], v[186:187], v[172:173] op_sel:[1,1,0] op_sel_hi:[1,0,1] neg_lo:[1,0,0]\n"
  "v_pk_fma_f32 v[184:185], v[184:185], v[186:187], v[174:175] op_sel:[1,1,0] op_sel_hi:[1,0,1] neg_lo:[1,0,0]\n"
  "v_pk_fma_f32 v[54:55], v[54:55], v[32:33], v[176:177] op_sel:[1,1,0] op_sel_hi:[1,0,1] neg_lo:[1,0,0]\n"
  "v_pk_fma_f32 v[198:199], v[198:199], v[32:33], v[178:179] op_sel:[1,1,0] op_sel_hi:[1,0,1] neg_lo:[1,0,0]\n"
  "v_pk_mul_f32 v[172:173], v[206:207], v[32:33] op_sel:[0,0] op_sel_hi:[0,1]\n"
  "v_cvt_pk_f16_f32 v98, v202, v203\n"
  "v_pk_mul_f32 v[174:175], v[28:29], v[186:187] op_sel:[0,0] op_sel_hi:[0,1]\n"
  "v_cvt_pk_f16_f32 v99, v206, v207\n"
  "v_pk_mul_f32 v[176:177], v[180:181], v[186:187] op_sel:[0,0] op_sel_hi:[0,1]\n"
  "ds_write_addtid_b32 v96 offset:10336\n"
  "ds_write_addtid_b32 v97 offset:10608\n"
  "ds_write_addtid_b32 v97 offset:11968\n"
  "ds_write_addtid_b32 v97 offset:12240\n"
  "ds_write_addtid_b32 v98 offset:12512\n"
  "ds_write_addtid_b32 v99 offset:12784\n"
  "buffer_store_dwordx4 v[168:171], v49, s[24:27], s46 offen offset:512 sc1\n"
  "v_cvt_pk_f16_f32 v100, v28, -v29\n"
  "v_cvt_pk_f16_f32 v101, v180, -v181\n"
  "v_pk_fma_f32 v[206:207], v[206:207], v[32:33], v[172:173] op_sel:[1,1,0] op_sel_hi:[1,0,1] neg_lo:[1,0,0]\n"
  "v_pk_fma_f32 v[28:29], v[28:29], v[186:187], v[174:175] op_sel:[1,1,0] op_sel_hi:[1,0,1] neg_lo:[1,0,0]\n"
  "v_pk_fma_f32 v[180:181], v[180:181], v[186:187], v[176:177] op_sel:[1,1,0] op_sel_hi:[1,0,1] neg_lo:[1,0,0]\n"
  "v_pk_mul_f32 v[172:173], v[182:183], v[186:187] op_sel:[0,0] op_sel_hi:[0,1]\n"
  "v_cvt_pk_f16_f32 v102, v182, -v183\n"
  "v_pk_mul_f32 v[174:175], v[184:185], v[186:187] op_sel:[0,0] op_sel_hi:[0,1]\n"
  "v_cvt_pk_f16_f32 v7, v184, -v185\n"
  "v_pk_mul_f32 v[176:177], v[54:55], v[32:33] op_sel:[0,0] op_sel_hi:[0,1]\n"
  "v_cvt_pk_f16_f32 v13, v54, v55\n"
  "v_pk_mul_f32 v[178:179], v[198:199], v[32:33] op_sel:[0,0] op_sel_hi:[0,1]\n"
  "v_cvt_pk_f16_f32 v18, v198, v199\n"
  "v_pk_fma_f32 v[182:183], v[182:183], v[186:187], v[172:173] op_sel:[1,1,0] op_sel_hi:[1,0,1] neg_lo:[1,0,0]\n"
  "ds_write_addtid_b32 v100 offset:2448\n"
  "ds_write_addtid_b32 v101 offset:2720\n"
  "ds_write_addtid_b32 v102 offset:2992\n"
  "ds_write_addtid_b32 v7 offset:3264\n"
  "ds_write_addtid_b32 v13 offset:13056\n"
  "ds_write_addtid_b32 v18 offset:13328\n"
  "s_add_u32 s46, s42, 229376\n"
  "buffer_store_dwordx4 v[168:171], v49, s[24:27], s46 offen sc1\n"
  "v_pk_fma_f32 v[184:185], v[184:185], v[186:187], v[174:175] op_sel:[1,1,0] op_sel_hi:[1,0,1] neg_lo:[1,0,0]\n"
  "v_pk_fma_f32 v[54:55], v[54:55], v[32:33], v[176:177] op_sel:[1,1,0] op_sel_hi:[1,0,1] neg_lo:[1,0,0]\n"
  "v_pk_fma_f32 v[198:199], v[198:199], v[32:33], v[178:179] op_sel:[1,1,0] op_sel_hi:[1,0,1] neg_lo:[1,0,0]\n"
  "v_pk_mul_f32 v[172:173], v[206:207], v[32:33] op_sel:[0,0] op_sel_hi:[0,1]\n"
  "v_cvt_pk_f16_f32 v58, v206, v207\n"
  "v_pk_mul_f32 v[174:175], v[28:29], v[186:187] op_sel:[0,0] op_sel_hi:[0,1]\n"
  "v_cvt_pk_f16_f32 v59, v28, -v29\n"
  "v_pk_mul_f32 v[176:177], v[180:181], v[186:187] op_sel:[0,0] op_sel_hi:[0,1]\n"
  "v_cvt_pk_f16_f32 v96, v180, -v181\n"
  "v_pk_fma_f32 v[206:207], v[206:207], v[32:33], v[172:173] op_sel:[1,1,0] op_sel_hi:[1,0,1] neg_lo:[1,0,0]\n"
  "v_pk_fma_f32 v[28:29], v[28:29], v[186:187], v[174:175] op_sel:[1,1,0] op_sel_hi:[1,0,1] neg_lo:[1,0,0]\n"
  "v_pk_fma_f32 v[180:181], v[180:181], v[186:187], v[176:177] op_sel:[1,1,0] op_sel_hi:[1,0,1] neg_lo:[1,0,0]\n"
  "v_pk_mul_f32 v[172:173], v[182:183], v[186:187] op_sel:[0,0] op_sel_hi:[0,1]\n"
  "v_cvt_pk_f16_f32 v97, v182, -v183\n"
  "v_pk_mul_f32 v[174:175], v[184:185], v[186:187] op_sel:[0,0] op_sel_hi:[0,1]\n"
  "v_cvt_pk_f16_f32 v98, v184, -v185\n"
  "v_pk_mul_f32 v[176:177], v[54:55], v[32:33] op_sel:[0,0] op_sel_hi:[0,1]\n"
  "v_cvt_pk_f16_f32 v99, v54, v55\n"
  "v_cvt_pk_f16_f32 v100, v198, v199\n"
  "ds_write_addtid_b32 v58 offset:13600\n"
  "ds_write_addtid_b32 v59 offset:3536\n"
  "ds_write_addtid_b32 v96 offset:3808\n"
  "ds_write_addtid_b32 v97 offset:4080\n"
  "ds_write_addtid_b32 v98 offset:4352\n"
  "ds_write_addtid_b32 v99 offset:14144\n"
  "buffer_store_dwordx4 v[168:171], v49, s[24:27], s46 offen offset:512 sc1\n"
  "v_pk_fma_f32 v[182:183], v[182:183], v[186:187], v[172:173] op_sel:[1,1,0] op_sel_hi:[1,0,1] neg_lo:[1,0,0]\n"
  "v_pk_fma_f32 v[184:185], v[184:185], v[186:187], v[174:175] op_sel:[1,1,0] op_sel_hi:[1,0,1] neg_lo:[1,0,0]\n"
  "v_pk_fma_f32 v[54:55], v[54:55], v[32:33], v[176:177] op_sel:[1,1,0] op_sel_hi:[1,0,1] neg_lo:[1,0,0]\n"
  "v_pk_mul_f32 v[172:173], v[206:207], v[32:33] op_sel:[0,0] op_sel_hi:[0,1]\n"
  "v_cvt_pk_f16_f32 v101, v206, v207\n"
  "v_pk_mul_f32 v[174:175], v[28:29], v[186:187] op_sel:[0,0] op_sel_hi:[0,1]\n"
  "v_cvt_pk_f16_f32 v102, v28, -v29\n"
  "v_pk_mul_f32 v[176:177], v[180:181], v[186:187] op_sel:[0,0] op_sel_hi:[0,1]\n"
  "v_cvt_pk_f16_f32 v7, v180, -v181\n"
  "v_pk_fma_f32 v[206:207], v[206:207], v[32:33], v[172:173] op_sel:[1,1,0] op_sel_hi:[1,0,1] neg_lo:[1,0,0]\n"
  "v_pk_fma_f32 v[28:29], v[28:29], v[186:187], v[174:175] op_sel:[1,1,0] op_sel_hi:[1,0,1] neg_lo:[1,0,0]\n"
  "v_pk_fma_f32 v[180:181], v[180:181], v[186:187], v[176:177] op_sel:[1,1,0] op_sel_hi:[1,0,1] neg_lo:[1,0,0]\n"
  "v_pk_mul_f32 v[172:173], v[182:183], v[186:187] op_sel:[0,0] op_sel_hi:[0,1]\n"
  "v_cvt_pk_f16_f32 v13, v182, -v183\n"
  "v_pk_mul_f32 v[174:175], v[184:185], v[186:187] op_sel:[0,0] op_sel_hi:[0,1]\n"
  "v_cvt_pk_f16_f32 v18, v184, -v185\n"
  "v_pk_mul_f32 v[176:177], v[54:55], v[32:33] op_sel:[0,0] op_sel_hi:[0,1]\n"
  "ds_write_addtid_b32 v100 offset:14416\n"
  "ds_write_addtid_b32 v101 offset:14688\n"
  "ds_write_addtid_b32 v102 offset:4624\n"
  "ds_write_addtid_b32 v7 offset:4896\n"
  "ds_write_addtid_b32 v13 offset:5168\n"
  "ds_write_addtid_b32 v18 offset:5440\n"
  "s_add_u32 s46, s42, 344064\n"
  "buffer_store_dwordx4 v[168:171], v49, s[24:27], s46 offen sc1\n"
  "v_cvt_pk_f16_f32 v58, v54, v55\n"
  "v_pk_fma_f32 v[182:183], v[182:183], v[186:187], v[172:173] op_sel:[1,1,0] op_sel_hi:[1,0,1] neg_lo:[1,0,0]\n"
  "v_pk_fma_f32 v[184:185], v[184:185], v[186:187], v[174:175] op_sel:[1,1,0] op_sel_hi:[1,0,1] neg_lo:[1,0,0]\n"
  "v_pk_fma_f32 v[54:55], v[54:55], v[32:33], v[176:177] op_sel:[1,1,0] op_sel_hi:[1,0,1] neg_lo:[1,0,0]\n"
  "v_pk_mul_f32 v[172:173], v[206:207], v[32:33] op_sel:[0,0] op_sel_hi:[0,1]\n"
  "v_cvt_pk_f16_f32 v59, v206, v207\n"
  "v_pk_mul_f32 v[174:175], v[28:29], v[186:187] op_sel:[0,0] op_sel_hi:[0,1]\n"
  "v_cvt_pk_f16_f32 v96, v28, -v29\n"
  "v_pk_mul_f32 v[176:177], v[180:181], v[186:187] op_sel:[0,0] op_sel_hi:[0,1]\n"
  "v_cvt_pk_f16_f32 v97, v180, -v181\n"
  "v_pk_fma_f32 v[206:207], v[206:207], v[32:33], v[172:173] op_sel:[1,1,0] op_sel_hi:[1,0,1] neg_lo:[1,0,0]\n"
  "v_pk_fma_f32 v[28:29], v[28:29], v[186:187], v[174:175] op_sel:[1,1,0] op_sel_hi:[1,0,1] neg_lo:[1,0,0]\n"
  "v_pk_fma_f32 v[180:181], v[180:181], v[186:187], v[176:177] op_sel:[1,1,0] op_sel_hi:[1,0,1] neg_lo:[1,0,0]\n"
  "v_pk_mul_f32 v[172:173], v[182:183], v[186:187] op_sel:[0,0] op_sel_hi:[0,1]\n"
  "v_cvt_pk_f16_f32 v98, v182, -v183\n"
  "v_pk_mul_f32 v[174:175], v[184:185], v[186:187] op_sel:[0,0] op_sel_hi:[0,1]\n"
  "v_cvt_pk_f16_f32 v99, v184, -v185\n"
  "v_pk_mul_f32 v[176:177], v[54:55], v[32:33] op_sel:[0,0] op_sel_hi:[0,1]\n"
  "ds_write_addtid_b32 v58 offset:13872\n"
  "ds_write_addtid_b32 v59 offset:15232\n"
  "ds_write_addtid_b32 v96 offset:5712\n"
  "ds_write_addtid_b32 v97 offset:5984\n"
  "ds_write_addtid_b32 v98 offset:6256\n"
  "ds_write_addtid_b32 v99 offset:6528\n"
  "buffer_store_dwordx4 v[168:171], v49, s[24:27], s46 offen offset:512 sc1\n"
  "v_cvt_pk_f16_f32 v100, v54, v55\n"
  "v_pk_fma_f32 v[182:183], v[182:183], v[186:187], v[172:173] op_sel:[1,1,0] op_sel_hi:[1,0,1] neg_lo:[1,0,0]\n"
  "v_pk_fma_f32 v[184:185], v[184:185], v[186:187], v[174:175] op_sel:[1,1,0] op_sel_hi:[1,0,1] neg_lo:[1,0,0]\n"
  "v_pk_fma_f32 v[54:55], v[54:55], v[32:33], v[176:177] op_sel:[1,1,0] op_sel_hi:[1,0,1] neg_lo:[1,0,0]\n"
  "v_pk_mul_f32 v[172:173], v[206:207], v[32:33] op_sel:[0,0] op_sel_hi:[0,1]\n"
  "v_cvt_pk_f16_f32 v101, v206, v207\n"
  "v_pk_mul_f32 v[174:175], v[28:29], v[186:187] op_sel:[0,0] op_sel_hi:[0,1]\n"
  "v_cvt_pk_f16_f32 v102, v28, -v29\n"
  "v_pk_mul_f32 v[176:177], v[180:181], v[186:187] op_sel:[0,0] op_sel_hi:[0,1]\n"
  "v_cvt_pk_f16_f32 v7, v180, -v181\n"
  "v_pk_fma_f32 v[206:207], v[206:207], v[32:33], v[172:173] op_sel:[1,1,0] op_sel_hi:[1,0,1] neg_lo:[1,0,0]\n"
  "v_pk_fma_f32 v[28:29], v[28:29], v[186:187], v[174:175] op_sel:[1,1,0] op_sel_hi:[1,0,1] neg_lo:[1,0,0]\n"
  "v_pk_fma_f32 v[180:181], v[180:181], v[186:187], v[176:177] op_sel:[1,1,0] op_sel_hi:[1,0,1] neg_lo:[1,0,0]\n"
  "v_pk_mul_f32 v[172:173], v[182:183], v[186:187] op_sel:[0,0] op_sel_hi:[0,1]\n"
  "v_cvt_pk_f16_f32 v13, v182, -v183\n"
  "v_pk_mul_f32 v[174:175], v[54:55], v[32:33] op_sel:[0,0] op_sel_hi:[0,1]\n"
  "v_cvt_pk_f16_f32 v18, v184, -v185\n"
  "v_cvt_pk_f16_f32 v58, v54, v55\n"
  "ds_write_addtid_b32 v100 offset:15504\n"
  "ds_write_addtid_b32 v101 offset:15776\n"
  "ds_write_addtid_b32 v102 offset:6800\n"
  "ds_write_addtid_b32 v7 offset:7072\n"
  "ds_write_addtid_b32 v13 offset:7344\n"
  "ds_write_addtid_b32 v18 offset:7616\n"
  "v_pk_fma_f32 v[182:183], v[182:183], v[186:187], v[172:173] op_sel:[1,1,0] op_sel_hi:[1,0,1] neg_lo:[1,0,0]\n"
  "v_pk_fma_f32 v[54:55], v[54:55], v[32:33], v[174:175] op_sel:[1,1,0] op_sel_hi:[1,0,1] neg_lo:[1,0,0]\n"
  "v_pk_mul_f32 v[172:173], v[206:207], v[32:33] op_sel:[0,0] op_sel_hi:[0,1]\n"
  "v_cvt_pk_f16_f32 v59, v206, v207\n"
  "v_cvt_pk_f16_f32 v96, v28, -v29\n"
  "v_cvt_pk_f16_f32 v97, v180, -v181\n"
  "v_pk_fma_f32 v[206:207], v[206:207], v[32:33], v[172:173] op_sel:[1,1,0] op_sel_hi:[1,0,1] neg_lo:[1,0,0]\n"
  "v_cvt_pk_f16_f32 v98, v182, -v183\n"
  "v_cvt_pk_f16_f32 v99, v54, v55\n"
  "v_cvt_pk_f16_f32 v100, v206, v207\n"
  "ds_write_addtid_b32 v58 offset:14960\n"
  "ds_write_addtid_b32 v59 offset:16320\n"
  "ds_write_addtid_b32 v96 offset:7888\n"
  "ds_write_addtid_b32 v97 offset:8160\n"
  "ds_write_addtid_b32 v98 offset:8432\n"
  "ds_write_addtid_b32 v99 offset:16592\n"
  "ds_write_addtid_b32 v100 offset:16864\n"
  "ds_read_b128 v[104:107], v19 offset:8704\n"
  "ds_read_b128 v[136:139], v19 offset:0\n"
  "ds_read_b128 v[108:111], v19 offset:8736\n"
  "ds_read_b128 v[140:143], v19 offset:32\n"
  "ds_read_b128 v[112:115], v19 offset:8768\n"
  "ds_read_b128 v[144:147], v19 offset:64\n"
  "v_pk_mul_f32 v[172:173], v[30:31], v[30:31] op_sel:[0,0] op_sel_hi:[0,1]\n"
  "v_pk_mul_f32 v[174:175], v[26:27], v[26:27] op_sel:[0,0] op_sel_hi:[0,1]\n"
  "v_pk_mul_f32 v[176:177], v[56:57], v[26:27] op_sel:[0,0] op_sel_hi:[0,1]\n"
  "v_pk_mul_f32 v[178:179], v[56:57], v[30:31] op_sel:[0,0] op_sel_hi:[0,1]\n"
  "v_pk_fma_f32 v[180:181], v[30:31], v[30:31], v[172:173] op_sel:[1,1,0] op_sel_hi:[1,0,1] neg_lo:[1,0,0]\n"
  "v_pk_fma_f32 v[188:189], v[26:27], v[26:27], v[174:175] op_sel:[1,1,0] op_sel_hi:[1,0,1] neg_lo:[1,0,0]\n"
  "v_pk_fma_f32 v[192:193], v[56:57], v[26:27], v[176:177] op_sel:[1,1,0] op_sel_hi:[1,0,1] neg_lo:[1,0,0]\n"
  "v_pk_fma_f32 v[206:207], v[56:57], v[30:31], v[178:179] op_sel:[1,1,0] op_sel_hi:[1,0,1] neg_lo:[1,0,0]\n"
  "v_pk_mul_f32 v[172:173], v[180:181], v[30:31] op_sel:[0,0] op_sel_hi:[0,1]\n"
  "v_pk_mul_f32 v[174:175], v[180:181], v[180:181] op_sel:[0,0] op_sel_hi:[0,1]\n"
  "v_pk_mul_f32 v[176:177], v[188:189], v[188:189] op_sel:[0,0] op_sel_hi:[0,1]\n"
  "v_pk_mul_f32 v[178:179], v[56:57], v[188:189] op_sel:[0,0] op_sel_hi:[0,1]\n"
  "v_pk_fma_f32 v[182:183], v[180:181], v[30:31], v[172:173] op_sel:[1,1,0] op_sel_hi:[1,0,1] neg_lo:[1,0,0]\n"
  "v_pk_fma_f32 v[186:187], v[180:181], v[180:181], v[174:175] op_sel:[1,1,0] op_sel_hi:[1,0,1] neg_lo:[1,0,0]\n"
  "v_pk_fma_f32 v[190:191], v[188:189], v[188:189], v[176:177] op_sel:[1,1,0] op_sel_hi:[1,0,1] neg_lo:[1,0,0]\n"
  "v_pk_fma_f32 v[194:195], v[56:57], v[188:189], v[178:179] op_sel:[1,1,0] op_sel_hi:[1,0,1] neg_lo:[1,0,0]\n"
  "v_pk_mul_f32 v[172:173], v[192:193], v[188:189] op_sel:[0,0] op_sel_hi:[0,1]\n"
  "v_pk_mul_f32 v[174:175], v[56:57], v[190:191] op_sel:[0,0] op_sel_hi:[0,1]\n"
  "v_pk_mul_f32 v[176:177], v[192:193], v[190:191] op_sel:[0,0] op_sel_hi:[0,1]\n"
  "v_pk_mul_f32 v[178:179], v[194:195], v[190:191] op_sel:[0,0] op_sel_hi:[0,1]\n"
  "v_pk_fma_f32 v[196:197], v[192:193], v[188:189], v[172:173] op_sel:[1,1,0] op_sel_hi:[1,0,1] neg_lo:[1,0,0]\n"
  "v_pk_fma_f32 v[198:199], v[56:57], v[190:191], v[174:175] op_sel:[1,1,0] op_sel_hi:[1,0,1] neg_lo:[1,0,0]\n"
  "v_pk_fma_f32 v[200:201], v[192:193], v[190:191], v[176:177] op_sel:[1,1,0] op_sel_hi:[1,0,1] neg_lo:[1,0,0]\n"
  "v_pk_fma_f32 v[202:203], v[194:195], v[190:191], v[178:179] op_sel:[1,1,0] op_sel_hi:[1,0,1] neg_lo:[1,0,0]\n"
  "s_waitcnt lgkmcnt(4)\n"
  "v_mfma_f32_32x32x16_f16 v[60:75], v[104:107], v[136:139], 0\n"
  "ds_read_b128 v[116:119], v19 offset:8800\n"
  "ds_read_b128 v[148:151], v19 offset:96\n"
  "v_cvt_pk_f16_f32 v101, v30, -v31\n"
  "v_cvt_pk_f16_f32 v102, v56, v57\n"
  "s_waitcnt lgkmcnt(4)\n"
  "v_mfma_f32_32x32x16_f16 v[60:75], v[108:111], v[140:143], v[60:75]\n"
  "ds_read_b128 v[120:123], v19 offset:8832\n"
  "ds_read_b128 v[152:155], v19 offset:128\n"
  "v_cvt_pk_f16_f32 v7, v180, -v181\n"
  "v_cvt_pk_f16_f32 v13, v192, v193\n"
  "s_waitcnt lgkmcnt(4)\n"
  "v_mfma_f32_32x32x16_f16 v[60:75], v[112:115], v[144:147], v[60:75]\n"
  "ds_read_b128 v[124:127], v19 offset:8864\n"
  "ds_read_b128 v[156:159], v19 offset:160\n"
  "v_cvt_pk_f16_f32 v18, v182, -v183\n"
  "v_cvt_pk_f16_f32 v58, v186, -v187\n"
  "s_waitcnt lgkmcnt(4)\n"
  "v_mfma_f32_32x32x16_f16 v[60:75], v[116:119], v[148:151], v[60:75]\n"
  "ds_read_b128 v[128:131], v19 offset:8896\n"
  "ds_read_b128 v[160:163], v19 offset:192\n"
  "v_cvt_pk_f16_f32 v59, v194, v195\n"
  "v_cvt_pk_f16_f32 v96, v196, v197\n"
  "s_waitcnt lgkmcnt(4)\n"
  "v_mfma_f32_32x32x16_f16 v[60:75], v[120:123], v[152:155], v[60:75]\n"
  "ds_read_b128 v[132:135], v19 offset:8928\n"
  "ds_read_b128 v[164:167], v19 offset:224\n"
  "v_cvt_pk_f16_f32 v97, v198, v199\n"
  "v_cvt_pk_f16_f32 v98, v200, v201\n"
  "s_waitcnt lgkmcnt(4)\n"
  "v_mfma_f32_32x32x16_f16 v[60:75], v[124:127], v[156:159], v[60:75]\n"
  "v_cvt_pk_f16_f32 v99, v202, v203\n"
  "s_waitcnt lgkmcnt(2)\n"
  "v_mfma_f32_32x32x16_f16 v[60:75], v[128:131], v[160:163], v[60:75]\n"
  "s_waitcnt lgkmcnt(0)\n"
  "v_mfma_f32_32x32x16_f16 v[60:75], v[132:135], v[164:167], v[60:75]\n"
  "ds_write_addtid_b32 v101 offset:272\n"
  "ds_write_addtid_b32 v102 offset:8704\n"
  "ds_write_addtid_b32 v7 offset:544\n"
  "ds_write_addtid_b32 v13 offset:8976\n"
  "ds_write_addtid_b32 v18 offset:816\n"
  "ds_write_addtid_b32 v58 offset:1088\n"
  "ds_write_addtid_b32 v59 offset:9248\n"
  "ds_write_addtid_b32 v96 offset:9520\n"
  "ds_write_addtid_b32 v97 offset:10880\n"
  "ds_write_addtid_b32 v98 offset:11152\n"
  "ds_write_addtid_b32 v99 offset:11424\n"
  "v_pk_mul_f32 v[172:173], v[196:197], v[190:191] op_sel:[0,0] op_sel_hi:[0,1]\n"
  "v_pk_mul_f32 v[174:175], v[30:31], v[186:187] op_sel:[0,0] op_sel_hi:[0,1]\n"
  "v_pk_mul_f32 v[176:177], v[180:181], v[186:187] op_sel:[0,0] op_sel_hi:[0,1]\n"
  "v_pk_mul_f32 v[178:179], v[182:183], v[186:187] op_sel:[0,0] op_sel_hi:[0,1]\n"
  "v_pk_fma_f32 v[204:205], v[196:197], v[190:191], v[172:173] op_sel:[1,1,0] op_sel_hi:[1,0,1] neg_lo:[1,0,0]\n"
  "v_pk_fma_f32 v[30:31], v[30:31], v[186:187], v[174:175] op_sel:[1,1,0] op_sel_hi:[1,0,1] neg_lo:[1,0,0]\n"
  "v_pk_fma_f32 v[180:181], v[180:181], v[186:187], v[176:177] op_sel:[1,1,0] op_sel_hi:[1,0,1] neg_lo:[1,0,0]\n"
  "v_pk_fma_f32 v[182:183], v[182:183], v[186:187], v[178:179] op_sel:[1,1,0] op_sel_hi:[1,0,1] neg_lo:[1,0,0]\n"
  "v_pk_mul_f32 v[172:173], v[186:187], v[186:187] op_sel:[0,0] op_sel_hi:[0,1]\n"
  "v_pk_mul_f32 v[174:175], v[56:57], v[34:35] op_sel:[0,0] op_sel_hi:[0,1]\n"
  "v_pk_mul_f32 v[176:177], v[194:195], v[34:35] op_sel:[0,0] op_sel_hi:[0,1]\n"
  "v_pk_mul_f32 v[178:179], v[198:199], v[34:35] op_sel:[0,0] op_sel_hi:[0,1]\n"
  "v_pk_fma_f32 v[184:185], v[186:187], v[186:187], v[172:173] op_sel:[1,1,0] op_sel_hi:[1,0,1] neg_lo:[1,0,0]\n"
  "v_pk_fma_f32 v[56:57], v[56:57], v[34:35], v[174:175] op_sel:[1,1,0] op_sel_hi:[1,0,1] neg_lo:[1,0,0]\n"
  "v_pk_fma_f32 v[194:195], v[194:195], v[34:35], v[176:177] op_sel:[1,1,0] op_sel_hi:[1,0,1] neg_lo:[1,0,0]\n"
  "v_pk_fma_f32 v[198:199], v[198:199], v[34:35], v[178:179] op_sel:[1,1,0] op_sel_hi:[1,0,1] neg_lo:[1,0,0]\n"
  "v_pk_mul_f32 v[172:173], v[202:203], v[34:35] op_sel:[0,0] op_sel_hi:[0,1]\n"
  "v_cvt_pk_f16_f32 v100, v204, v205\n"
  "v_pk_mul_f32 v[174:175], v[206:207], v[34:35] op_sel:[0,0] op_sel_hi:[0,1]\n"
  "v_cvt_pk_f16_f32 v101, v30, -v31\n"
  "v_pk_mul_f32 v[176:177], v[30:31], v[186:187] op_sel:[0,0] op_sel_hi:[0,1]\n"
  "v_cvt_pk_f16_f32 v102, v180, -v181\n"
  "v_pk_mul_f32 v[178:179], v[180:181], v[186:187] op_sel:[0,0] op_sel_hi:[0,1]\n"
  "v_cvt_pk_f16_f32 v7, v182, -v183\n"
  "v_pk_fma_f32 v[202:203], v[202:203], v[34:35], v[172:173] op_sel:[1,1,0] op_sel_hi:[1,0,1] neg_lo:[1,0,0]\n"
  "v_pk_fma_f32 v[206:207], v[206:207], v[34:35], v[174:175] op_sel:[1,1,0] op_sel_hi:[1,0,1] neg_lo:[1,0,0]\n"
  "v_pk_fma_f32 v[30:31], v[30:31], v[186:187], v[176:177] op_sel:[1,1,0] op_sel_hi:[1,0,1] neg_lo:[1,0,0]\n"
  "v_pk_fma_f32 v[180:181], v[180:181], v[186:187], v[178:179] op_sel:[1,1,0] op_sel_hi:[1,0,1] neg_lo:[1,0,0]\n"
  "v_pk_mul_f32 v[172:173], v[182:183], v[186:187] op_sel:[0,0] op_sel_hi:[0,1]\n"
  "v_cvt_pk_f16_f32 v13, v184, -v185\n"
  "v_pk_mul_f32 v[174:175], v[184:185], v[186:187] op_sel:[0,0] op_sel_hi:[0,1]\n"
  "v_cvt_pk_f16_f32 v18, v56, v57\n"
  "v_pk_mul_f32 v[176:177], v[56:57], v[34:35] op_sel:[0,0] op_sel_hi:[0,1]\n"
  "ds_write_addtid_b32 v100 offset:11696\n"
  "ds_write_addtid_b32 v101 offset:1360\n"
  "ds_write_addtid_b32 v102 offset:1632\n"
  "ds_write_addtid_b32 v7 offset:1904\n"
  "ds_write_addtid_b32 v13 offset:2176\n"
  "ds_write_addtid_b32 v18 offset:9792\n"
  "v_cvt_pk_f16_f32 v58, v194, v195\n"
  "v_pk_mul_f32 v[178:179], v[198:199], v[34:35] op_sel:[0,0] op_sel_hi:[0,1]\n"
  "v_cvt_pk_f16_f32 v59, v198, v199\n"
  "v_pk_fma_f32 v[182:183], v[182:183], v[186:187], v[172:173] op_sel:[1,1,0] op_sel_hi:[1,0,1] neg_lo:[1,0,0]\n"
  "v_pk_fma_f32 v[184:185], v[184:185], v[186:187], v[174:175] op_sel:[1,1,0] op_sel_hi:[1,0,1] neg_lo:[1,0,0]\n"
  "v_pk_fma_f32 v[56:57], v[56:57], v[34:35], v[176:177] op_sel:[1,1,0] op_sel_hi:[1,0,1] neg_lo:[1,0,0]\n"
  "v_pk_fma_f32 v[198:199], v[198:199], v[34:35], v[178:179] op_sel:[1,1,0] op_sel_hi:[1,0,1] neg_lo:[1,0,0]\n"
  "v_pk_mul_f32 v[172:173], v[206:207], v[34:35] op_sel:[0,0] op_sel_hi:[0,1]\n"
  "v_cvt_pk_f16_f32 v96, v202, v203\n"
  "v_pk_mul_f32 v[174:175], v[30:31], v[186:187] op_sel:[0,0] op_sel_hi:[0,1]\n"
  "ds_write_addtid_b32 v18 offset:10064\n"
  "ds_write_addtid_b32 v58 offset:10336\n"
  "ds_write_addtid_b32 v59 offset:10608\n"
  "ds_write_addtid_b32 v59 offset:11968\n"
  "ds_write_addtid_b32 v59 offset:12240\n"
  "ds_write_addtid_b32 v96 offset:12512\n"
  "v_cvt_pk_f16_f32 v97, v206, v207\n"
  "v_pk_mul_f32 v[176:177], v[180:181], v[186:187] op_sel:[0,0] op_sel_hi:[0,1]\n"
  "v_cvt_pk_f16_f32 v98, v30, -v31\n"
  "v_cvt_pk_f16_f32 v99, v180, -v181\n"
  "v_pk_fma_f32 v[206:207], v[206:207], v[34:35], v[172:173] op_sel:[1,1,0] op_sel_hi:[1,0,1] neg_lo:[1,0,0]\n"
  "v_pk_fma_f32 v[30:31], v[30:31], v[186:187], v[174:175] op_sel:[1,1,0] op_sel_hi:[1,0,1] neg_lo:[1,0,0]\n"
  "v_pk_fma_f32 v[180:181], v[180:181], v[186:187], v[176:177] op_sel:[1,1,0] op_sel_hi:[1,0,1] neg_lo:[1,0,0]\n"
  "v_pk_mul_f32 v[172:173], v[182:183], v[186:187] op_sel:[0,0] op_sel_hi:[0,1]\n"
  "v_cvt_pk_f16_f32 v100, v182, -v183\n"
  "v_pk_mul_f32 v[174:175], v[184:185], v[186:187] op_sel:[0,0] op_sel_hi:[0,1]\n"
  "v_cvt_pk_f16_f32 v101, v184, -v185\n"
  "v_pk_mul_f32 v[176:177], v[56:57], v[34:35] op_sel:[0,0] op_sel_hi:[0,1]\n"
  "v_cvt_pk_f16_f32 v102, v56, v57\n"
  "v_pk_mul_f32 v[178:179], v[198:199], v[34:35] op_sel:[0,0] op_sel_hi:[0,1]\n"
  "ds_write_addtid_b32 v97 offset:12784\n"
  "ds_write_addtid_b32 v98 offset:2448\n"
  "ds_write_addtid_b32 v99 offset:2720\n"
  "ds_write_addtid_b32 v100 offset:2992\n"
  "ds_write_addtid_b32 v101 offset:3264\n"
  "ds_write_addtid_b32 v102 offset:13056\n"
  "v_cvt_pk_f16_f32 v7, v198, v199\n"
  "v_pk_fma_f32 v[182:183], v[182:183], v[186:187], v[172:173] op_sel:[1,1,0] op_sel_hi:[1,0,1] neg_lo:[1,0,0]\n"
  "v_pk_fma_f32 v[184:185], v[184:185], v[186:187], v[174:175] op_sel:[1,1,0] op_sel_hi:[1,0,1] neg_lo:[1,0,0]\n"
  "v_pk_fma_f32 v[56:57], v[56:57], v[34:35], v[176:177] op_sel:[1,1,0] op_sel_hi:[1,0,1] neg_lo:[1,0,0]\n"
  "v_pk_fma_f32 v[198:199], v[198:199], v[34:35], v[178:179] op_sel:[1,1,0] op_sel_hi:[1,0,1] neg_lo:[1,0,0]\n"
  "v_pk_mul_f32 v[172:173], v[206:207], v[34:35] op_sel:[0,0] op_sel_hi:[0,1]\n"
  "v_cvt_pk_f16_f32 v13, v206, v207\n"
  "v_pk_mul_f32 v[174:175], v[30:31], v[186:187] op_sel:[0,0] op_sel_hi:[0,1]\n"
  "v_cvt_pk_f16_f32 v18, v30, -v31\n"
  "v_pk_mul_f32 v[176:177], v[180:181], v[186:187] op_sel:[0,0] op_sel_hi:[0,1]\n"
  "v_cvt_pk_f16_f32 v58, v180, -v181\n"
  "v_pk_fma_f32 v[206:207], v[206:207], v[34:35], v[172:173] op_sel:[1,1,0] op_sel_hi:[1,0,1] neg_lo:[1,0,0]\n"
  "v_pk_fma_f32 v[30:31], v[30:31], v[186:187], v[174:175] op_sel:[1,1,0] op_sel_hi:[1,0,1] neg_lo:[1,0,0]\n"
  "v_pk_fma_f32 v[180:181], v[180:181], v[186:187], v[176:177] op_sel:[1,1,0] op_sel_hi:[1,0,1] neg_lo:[1,0,0]\n"
  "v_pk_mul_f32 v[172:173], v[182:183], v[186:187] op_sel:[0,0] op_sel_hi:[0,1]\n"
  "v_cvt_pk_f16_f32 v59, v182, -v183\n"
  "v_pk_mul_f32 v[174:175], v[184:185], v[186:187] op_sel:[0,0] op_sel_hi:[0,1]\n"
  "v_cvt_pk_f16_f32 v96, v184, -v185\n"
  "v_pk_mul_f32 v[176:177], v[56:57], v[34:35] op_sel:[0,0] op_sel_hi:[0,1]\n"
  "ds_write_addtid_b32 v7 offset:13328\n"
  "ds_write_addtid_b32 v13 offset:13600\n"
  "ds_write_addtid_b32 v18 offset:3536\n"
  "ds_write_addtid_b32 v58 offset:3808\n"
  "ds_write_addtid_b32 v59 offset:4080\n"
  "ds_write_addtid_b32 v96 offset:4352\n"
  "v_cvt_pk_f16_f32 v97, v56, v57\n"
  "v_cvt_pk_f16_f32 v98, v198, v199\n"
  "v_pk_fma_f32 v[182:183], v[182:183], v[186:187], v[172:173] op_sel:[1,1,0] op_sel_hi:[1,0,1] neg_lo:[1,0,0]\n"
  "v_pk_fma_f32 v[184:185], v[184:185], v[186:187], v[174:175] op_sel:[1,1,0] op_sel_hi:[1,0,1] neg_lo:[1,0,0]\n"
  "v_pk_fma_f32 v[56:57], v[56:57], v[34:35], v[176:177] op_sel:[1,1,0] op_sel_hi:[1,0,1] neg_lo:[1,0,0]\n"
  "v_pk_mul_f32 v[172:173], v[206:207], v[34:35] op_sel:[0,0] op_sel_hi:[0,1]\n"
  "v_cvt_pk_f16_f32 v99, v206, v207\n"
  "v_pk_mul_f32 v[174:175], v[30:31], v[186:187] op_sel:[0,0] op_sel_hi:[0,1]\n"
  "v_cvt_pk_f16_f32 v100, v30, -v31\n"
  "v_pk_mul_f32 v[176:177], v[180:181], v[186:187] op_sel:[0,0] op_sel_hi:[0,1]\n"
  "v_cvt_pk_f16_f32 v101, v180, -v181\n"
  "v_pk_fma_f32 v[206:207], v[206:207], v[34:35], v[172:173] op_sel:[1,1,0] op_sel_hi:[1,0,1] neg_lo:[1,0,0]\n"
  "v_pk_fma_f32 v[30:31], v[30:31], v[186:187], v[174:175] op_sel:[1,1,0] op_sel_hi:[1,0,1] neg_lo:[1,0,0]\n"
  "v_pk_fma_f32 v[180:181], v[180:181], v[186:187], v[176:177] op_sel:[1,1,0] op_sel_hi:[1,0,1] neg_lo:[1,0,0]\n"
  "v_pk_mul_f32 v[172:173], v[182:183], v[186:187] op_sel:[0,0] op_sel_hi:[0,1]\n"
  "v_cvt_pk_f16_f32 v102, v182, -v183\n"
  "v_pk_mul_f32 v[174:175], v[184:185], v[186:187] op_sel:[0,0] op_sel_hi:[0,1]\n"
  "ds_write_addtid_b32 v97 offset:14144\n"
  "ds_write_addtid_b32 v98 offset:14416\n"
  "ds_write_addtid_b32 v99 offset:14688\n"
  "ds_write_addtid_b32 v100 offset:4624\n"
  "ds_write_addtid_b32 v101 offset:4896\n"
  "ds_write_addtid_b32 v102 offset:5168\n"
  "v_cvt_pk_f16_f32 v7, v184, -v185\n"
  "v_pk_mul_f32 v[176:177], v[56:57], v[34:35] op_sel:[0,0] op_sel_hi:[0,1]\n"
  "v_cvt_pk_f16_f32 v13, v56, v57\n"
  "v_pk_fma_f32 v[182:183], v[182:183], v[186:187], v[172:173] op_sel:[1,1,0] op_sel_hi:[1,0,1] neg_lo:[1,0,0]\n"
  "v_pk_fma_f32 v[184:185], v[184:185], v[186:187], v[174:175] op_sel:[1,1,0] op_sel_hi:[1,0,1] neg_lo:[1,0,0]\n"
  "v_pk_fma_f32 v[56:57], v[56:57], v[34:35], v[176:177] op_sel:[1,1,0] op_sel_hi:[1,0,1] neg_lo:[1,0,0]\n"
  "v_pk_mul_f32 v[172:173], v[206:207], v[34:35] op_sel:[0,0] op_sel_hi:[0,1]\n"
  "v_cvt_pk_f16_f32 v18, v206, v207\n"
  "v_pk_mul_f32 v[174:175], v[30:31], v[186:187] op_sel:[0,0] op_sel_hi:[0,1]\n"
  "v_cvt_pk_f16_f32 v58, v30, -v31\n"
  "v_pk_mul_f32 v[176:177], v[180:181], v[186:187] op_sel:[0,0] op_sel_hi:[0,1]\n"
  "v_cvt_pk_f16_f32 v59, v180, -v181\n"
  "v_pk_fma_f32 v[206:207], v[206:207], v[34:35], v[172:173] op_sel:[1,1,0] op_sel_hi:[1,0,1] neg_lo:[1,0,0]\n"
  "v_pk_fma_f32 v[30:31], v[30:31], v[186:187], v[174:175] op_sel:[1,1,0] op_sel_hi:[1,0,1] neg_lo:[1,0,0]\n"
  "v_pk_fma_f32 v[180:181], v[180:181], v[186:187], v[176:177] op_sel:[1,1,0] op_sel_hi:[1,0,1] neg_lo:[1,0,0]\n"
  "v_pk_mul_f32 v[172:173], v[182:183], v[186:187] op_sel:[0,0] op_sel_hi:[0,1]\n"
  "v_cvt_pk_f16_f32 v96, v182, -v183\n"
  "v_pk_mul_f32 v[174:175], v[184:185], v[186:187] op_sel:[0,0] op_sel_hi:[0,1]\n"
  "ds_write_addtid_b32 v7 offset:5440\n"
  "ds_write_addtid_b32 v13 offset:13872\n"
  "ds_write_addtid_b32 v18 offset:15232\n"
  "ds_write_addtid_b32 v58 offset:5712\n"
  "ds_write_addtid_b32 v59 offset:5984\n"
  "ds_write_addtid_b32 v96 offset:6256\n"
  "v_cvt_pk_f16_f32 v97, v184, -v185\n"
  "v_pk_mul_f32 v[176:177], v[56:57], v[34:35] op_sel:[0,0] op_sel_hi:[0,1]\n"
  "v_cvt_pk_f16_f32 v98, v56, v57\n"
  "v_pk_fma_f32 v[182:183], v[182:183], v[186:187], v[172:173] op_sel:[1,1,0] op_sel_hi:[1,0,1] neg_lo:[1,0,0]\n"
  "v_pk_fma_f32 v[184:185], v[184:185], v[186:187], v[174:175] op_sel:[1,1,0] op_sel_hi:[1,0,1] neg_lo:[1,0,0]\n"
  "v_pk_fma_f32 v[56:57], v[56:57], v[34:35], v[176:177] op_sel:[1,1,0] op_sel_hi:[1,0,1] neg_lo:[1,0,0]\n"
  "v_pk_mul_f32 v[172:173], v[206:207], v[34:35] op_sel:[0,0] op_sel_hi:[0,1]\n"
  "v_cvt_pk_f16_f32 v99, v206, v207\n"
  "v_pk_mul_f32 v[174:175], v[30:31], v[186:187] op_sel:[0,0] op_sel_hi:[0,1]\n"
  "v_cvt_pk_f16_f32 v100, v30, -v31\n"
  "v_pk_mul_f32 v[176:177], v[180:181], v[186:187] op_sel:[0,0] op_sel_hi:[0,1]\n"
  "v_cvt_pk_f16_f32 v101, v180, -v181\n"
  "v_pk_fma_f32 v[206:207], v[206:207], v[34:35], v[172:173] op_sel:[1,1,0] op_sel_hi:[1,0,1] neg_lo:[1,0,0]\n"
  "v_pk_fma_f32 v[30:31], v[30:31], v[186:187], v[174:175] op_sel:[1,1,0] op_sel_hi:[1,0,1] neg_lo:[1,0,0]\n"
  "v_pk_fma_f32 v[180:181], v[180:181], v[186:187], v[176:177] op_sel:[1,1,0] op_sel_hi:[1,0,1] neg_lo:[1,0,0]\n"
  "v_pk_mul_f32 v[172:173], v[182:183], v[186:187] op_sel:[0,0] op_sel_hi:[0,1]\n"
  "v_cvt_pk_f16_f32 v102, v182, -v183\n"
  "v_pk_mul_f32 v[174:175], v[56:57], v[34:35] op_sel:[0,0] op_sel_hi:[0,1]\n"
  "ds_write_addtid_b32 v97 offset:6528\n"
  "ds_write_addtid_b32 v98 offset:15504\n"
  "ds_write_addtid_b32 v99 offset:15776\n"
  "ds_write_addtid_b32 v100 offset:6800\n"
  "ds_write_addtid_b32 v101 offset:7072\n"
  "ds_write_addtid_b32 v102 offset:7344\n"
  "v_cvt_pk_f16_f32 v7, v184, -v185\n"
  "v_cvt_pk_f16_f32 v13, v56, v57\n"
  "v_pk_fma_f32 v[182:183], v[182:183], v[186:187], v[172:173] op_sel:[1,1,0] op_sel_hi:[1,0,1] neg_lo:[1,0,0]\n"
  "v_pk_fma_f32 v[56:57], v[56:57], v[34:35], v[174:175] op_sel:[1,1,0] op_sel_hi:[1,0,1] neg_lo:[1,0,0]\n"
  "v_pk_mul_f32 v[172:173], v[206:207], v[34:35] op_sel:[0,0] op_sel_hi:[0,1]\n"
  "v_cvt_pk_f16_f32 v18, v206, v207\n"
  "v_cvt_pk_f16_f32 v58, v30, -v31\n"
  "v_cvt_pk_f16_f32 v59, v180, -v181\n"
  "v_pk_fma_f32 v[206:207], v[206:207], v[34:35], v[172:173] op_sel:[1,1,0] op_sel_hi:[1,0,1] neg_lo:[1,0,0]\n"
  "v_cvt_pk_f16_f32 v96, v182, -v183\n"
  "v_cvt_pk_f16_f32 v97, v56, v57\n"
  "ds_write_addtid_b32 v7 offset:7616\n"
  "ds_write_addtid_b32 v13 offset:14960\n"
  "ds_write_addtid_b32 v18 offset:16320\n"
  "ds_write_addtid_b32 v58 offset:7888\n"
  "ds_write_addtid_b32 v59 offset:8160\n"
  "ds_write_addtid_b32 v96 offset:8432\n"
  "v_cvt_pk_f16_f32 v98, v206, v207\n"
  "ds_write_addtid_b32 v97 offset:16592\n"
  "ds_write_addtid_b32 v98 offset:16864\n"
  "ds_read_b128 v[104:107], v19 offset:8704\n"
  "ds_read_b128 v[136:139], v19 offset:0\n"
  "ds_read_b128 v[108:111], v19 offset:8736\n"
  "ds_read_b128 v[140:143], v19 offset:32\n"
  "ds_read_b128 v[112:115], v19 offset:8768\n"
  "ds_read_b128 v[144:147], v19 offset:64\n"
  "buffer_load_dwordx4 v[14:17], v38, s[24:27], 0 offen sc1\n"
  "buffer_load_dwordx4 v[80:83], v39, s[24:27], 0 offen sc1\n"
  "buffer_load_dwordx4 v[20:23], v38, s[24:27], s22 offen sc1\n"
  "buffer_load_dwordx4 v[84:87], v39, s[24:27], s22 offen sc1\n"
  "buffer_load_dwordx4 v[50:53], v38, s[24:27], s23 offen sc1\n"
  "buffer_load_dwordx4 v[88:91], v39, s[24:27], s23 offen sc1\n"
  "buffer_load_dwordx4 v[76:79], v38, s[24:27], s43 offen sc1\n"
  "buffer_load_dwordx4 v[92:95], v39, s[24:27], s43 offen sc1\n"
  "s_waitcnt lgkmcnt(4)\n"
  "v_mfma_f32_32x32x16_f16 v[60:75], v[104:107], v[136:139], v[60:75]\n"
  "ds_read_b128 v[116:119], v19 offset:8800\n"
  "ds_read_b128 v[148:151], v19 offset:96\n"
  "s_waitcnt lgkmcnt(4)\n"
  "v_mfma_f32_32x32x16_f16 v[60:75], v[108:111], v[140:143], v[60:75]\n"
  "ds_read_b128 v[120:123], v19 offset:8832\n"
  "ds_read_b128 v[152:155], v19 offset:128\n"
  "s_waitcnt lgkmcnt(4)\n"
  "v_mfma_f32_32x32x16_f16 v[60:75], v[112:115], v[144:147], v[60:75]\n"
  "ds_read_b128 v[124:127], v19 offset:8864\n"
  "ds_read_b128 v[156:159], v19 offset:160\n"
  "s_waitcnt lgkmcnt(4)\n"
  "v_mfma_f32_32x32x16_f16 v[60:75], v[116:119], v[148:151], v[60:75]\n"
  "ds_read_b128 v[128:131], v19 offset:8896\n"
  "ds_read_b128 v[160:163], v19 offset:192\n"
  "s_waitcnt lgkmcnt(4)\n"
  "v_mfma_f32_32x32x16_f16 v[60:75], v[120:123], v[152:155], v[60:75]\n"
  "ds_read_b128 v[132:135], v19 offset:8928\n"
  "ds_read_b128 v[164:167], v19 offset:224\n"
  "s_waitcnt lgkmcnt(4)\n"
  "v_mfma_f32_32x32x16_f16 v[60:75], v[124:127], v[156:159], v[60:75]\n"
  "s_waitcnt lgkmcnt(2)\n"
  "v_mfma_f32_32x32x16_f16 v[60:75], v[128:131], v[160:163], v[60:75]\n"
  "s_waitcnt lgkmcnt(0)\n"
  "v_mfma_f32_32x32x16_f16 v[60:75], v[132:135], v[164:167], v[60:75]\n"
  "s_add_u32 s4, s38, 131072\n"
  "v_lshl_add_u32 v6, v2, 4, s4\n"
  "s_xor_b32 s4, s4, 4096\n"
  "s_mul_i32 s5, s14, 16640\n"
  "s_add_u32 s5, s5, 64\n"
  "v_lshl_add_u32 v9, v3, 4, s5\n"
  "v_lshl_add_u32 v7, v4, 9, v9\n"
  "v_lshl_add_u32 v13, v4, 10, v9\n"
  "v_and_b32 v10, 1, v2\n"
  "v_cmp_eq_u32 vcc, 1, v10\n"
  "s_cmp_lg_u32 s15, 0\n"
  "s_cbranch_scc1 L10_%=\n"
  "ds_write_b128 v6, v[68:71]\n"
  "ds_write_b32 v6, v72 offset:1024\n"
  "v_lshl_add_u32 v6, v2, 4, s4\n"
  "s_waitcnt lgkmcnt(0)\n"
  "s_barrier\n"
  "ds_read_b128 v[24:27], v6\n"
  "ds_read_b128 v[28:31], v6 offset:1024\n"
  "ds_read_b64 v[18:19], v6 offset:2048\n"
  "v_lshrrev_b32 v11, 2, v2\n"
  "v_mul_u32_u24 v11, 4160, v11\n"
  "v_lshl_add_u32 v11, v8, 4, v11\n"
  "s_sub_u32 s5, s5, 64\n"
  "v_add_u32 v11, s5, v11\n"
  "s_waitcnt lgkmcnt(2)\n"
  "v_pk_add_f32 v[24:25], v[24:25], v[60:61]\n"
  "v_pk_add_f32 v[26:27], v[26:27], v[62:63]\n"
  "s_waitcnt lgkmcnt(1)\n"
  "v_pk_add_f32 v[28:29], v[28:29], v[64:65]\n"
  "v_pk_add_f32 v[30:31], v[30:31], v[66:67]\n"
  "v_cvt_pk_f16_f32 v32, v24, v25\n"
  "v_cvt_pk_f16_f32 v33, v26, v27\n"
  "v_cvt_pk_f16_f32 v34, v28, v29\n"
  "v_cvt_pk_f16_f32 v35, v30, v31\n"
  "v_cvt_pk_f16_f32 v99, v31, v31\n"
  "v_cvt_pk_f16_f32 v24, v31, v31\n"
  "s_waitcnt lgkmcnt(0)\n"
  "v_add_f32 v18, v18, v73\n"
  "v_add_f32 v19, v19, v74\n"
  "v_mov_b32_dpp v99, v32 wave_shl:1 row_mask:0xf bank_mask:0xf\n"
  "v_mov_b32_dpp v24, v33 wave_shl:1 row_mask:0xf bank_mask:0xf\n"
  "v_readlane_b32 s6, v32, 0\n"
  "v_readlane_b32 s7, v33, 0\n"
  "v_mov_b32 v96, v33\n"
  "v_alignbit_b32 v54, v33, v32, 16\n"
  "v_mov_b32 v97, v34\n"
  "v_alignbit_b32 v55, v34, v33, 16\n"
  "v_mov_b32 v98, v35\n"
  "v_alignbit_b32 v56, v35, v34, 16\n"
  "v_alignbit_b32 v57, v99, v35, 16\n"
  "v_alignbit_b32 v103, v24, v99, 16\n"
  "v_mov_b32 v100, v55\n"
  "v_mov_b32 v101, v56\n"
  "v_mov_b32 v102, v57\n"
  "v_cndmask_b32 v10, v18, v19, vcc\n"
  "v_cvt_pk_f16_f32 v104, v18, v18\n"
  "v_cvt_pk_f16_f32 v105, v18, v18\n"
  "v_cvt_pk_f16_f32 v108, v18, v18\n"
  "v_cvt_pk_f16_f32 v112, v18, v18\n"
  "v_cvt_pk_f16_f32 v107, v19, v19\n"
  "v_cvt_pk_f16_f32 v110, v19, v19\n"
  "v_cvt_pk_f16_f32 v111, v19, v19\n"
  "v_cvt_pk_f16_f32 v114, v19, v19\n"
  "v_cvt_pk_f16_f32 v115, v19, v19\n"
  "v_cvt_pk_f16_f32 v117, v19, v19\n"
  "v_cvt_pk_f16_f32 v118, v19, v19\n"
  "v_cvt_pk_f16_f32 v119, v19, v19\n"
  "v_cvt_pk_f16_f32 v106, v10, v19\n"
  "v_cvt_pk_f16_f32 v113, v10, v19\n"
  "v_cvt_pk_f16_f32 v109, v18, v10\n"
  "v_cvt_pk_f16_f32 v116, v18, v10\n"
  "v_mov_b32 v28, 0\n"
  "v_mov_b32 v29, 0\n"
  "v_mov_b32 v30, 0\n"
  "v_mov_b32 v31, 0\n"
  "s_lshr_b32 s4, s6, 16\n"
  "s_lshl_b32 s7, s7, 16\n"
  "s_or_b32 s7, s7, s4\n"
  "s_lshl_b32 s4, s6, 16\n"
  "v_writelane_b32 v30, s4, 15\n"
  "v_writelane_b32 v31, s4, 7\n"
  "v_writelane_b32 v31, s6, 11\n"
  "v_writelane_b32 v31, s7, 15\n"
  "v_add_u32 v13, 2560, v13\n"
  "s_mov_b64 exec, 0xffff\n"
  "ds_write_b128 v11, v[28:31]\n"
  "s_mov_b64 exec, -1\n"
  "s_branch L11_%=\n"
  "L10_%=:\n"
  "v_mov_b32 v18, v73\n"
  "v_mov_b32 v19, v74\n"
  "ds_write_b128 v6, v[60:63]\n"
  "ds_write_b128 v6, v[64:67] offset:1024\n"
  "ds_write_b64 v6, v[18:19] offset:2048\n"
  "v_lshl_add_u32 v6, v2, 4, s4\n"
  "s_waitcnt lgkmcnt(0)\n"
  "s_barrier\n"
  "ds_read_b128 v[24:27], v6\n"
  "ds_read_b32 v28, v6 offset:1024\n"
  "v_mov_b32 v104, 0\n"
  "v_mov_b32 v105, 0\n"
  "v_mov_b32 v106, 0\n"
  "v_mov_b32 v107, 0\n"
  "v_and_b32 v11, 63, v1\n"
  "s_lshl_b32 s4, s3, 5\n"
  "s_and_b32 s4, s4, 64\n"
  "v_add_u32 v11, s4, v11\n"
  "v_mov_b32 v10, 90\n"
  "v_cmp_gt_u32 s[46:47], v10, v11\n"
  "v_lshlrev_b32 v11, 4, v11\n"
  "s_mov_b64 exec, s[46:47]\n"
  "ds_write_b128 v11, v[104:107] offset:33280\n"
  "ds_write_b128 v11, v[104:107] offset:40864\n"
  "s_mov_b64 exec, -1\n"
  "s_waitcnt lgkmcnt(3)\n"
  "v_pk_add_f32 v[24:25], v[24:25], v[68:69]\n"
  "v_pk_add_f32 v[26:27], v[26:27], v[70:71]\n"
  "v_cvt_pk_f16_f32 v32, v24, v24\n"
  "v_cvt_pk_f16_f32 v33, v24, v25\n"
  "v_cvt_pk_f16_f32 v34, v25, v25\n"
  "v_cvt_pk_f16_f32 v35, v26, v26\n"
  "v_cvt_pk_f16_f32 v54, v24, v24\n"
  "v_cvt_pk_f16_f32 v55, v25, v25\n"
  "v_cvt_pk_f16_f32 v56, v25, v26\n"
  "v_cvt_pk_f16_f32 v57, v26, v26\n"
  "v_cvt_pk_f16_f32 v96, v24, v25\n"
  "v_cvt_pk_f16_f32 v97, v25, v25\n"
  "v_cvt_pk_f16_f32 v98, v26, v26\n"
  "v_cvt_pk_f16_f32 v99, v26, v26\n"
  "v_cvt_pk_f16_f32 v100, v25, v25\n"
  "v_cvt_pk_f16_f32 v101, v25, v26\n"
  "v_cvt_pk_f16_f32 v102, v26, v26\n"
  "v_cvt_pk_f16_f32 v103, v26, v26\n"
  "v_add_u32 v7, 1024, v7\n"
  "s_waitcnt lgkmcnt(2)\n"
  "v_add_f32 v28, v28, v72\n"
  "v_cndmask_b32 v10, v27, v28, vcc\n"
  "v_cvt_pk_f16_f32 v104, v27, v27\n"
  "v_cvt_pk_f16_f32 v105, v27, v27\n"
  "v_cvt_pk_f16_f32 v108, v27, v27\n"
  "v_cvt_pk_f16_f32 v112, v27, v27\n"
  "v_cvt_pk_f16_f32 v107, v28, v28\n"
  "v_cvt_pk_f16_f32 v110, v28, v28\n"
  "v_cvt_pk_f16_f32 v111, v28, v28\n"
  "v_cvt_pk_f16_f32 v114, v28, v28\n"
  "v_cvt_pk_f16_f32 v115, v28, v28\n"
  "v_cvt_pk_f16_f32 v117, v28, v28\n"
  "v_cvt_pk_f16_f32 v118, v28, v28\n"
  "v_cvt_pk_f16_f32 v119, v28, v28\n"
  "v_cvt_pk_f16_f32 v106, v10, v28\n"
  "v_cvt_pk_f16_f32 v113, v10, v28\n"
  "v_cvt_pk_f16_f32 v109, v27, v10\n"
  "v_cvt_pk_f16_f32 v116, v27, v10\n"
  "v_add_u32 v13, 2048, v13\n"
  "L11_%=:\n"
  "s_lshl_b32 s4, s14, 2\n"
  "v_mov_b32 v10, s4\n"
  "ds_write_b32 v10, v5 offset:48448\n"
  "ds_write_b128 v7, v[32:35] offset:0\n"
  "ds_write_b128 v7, v[54:57] offset:4160\n"
  "s_waitcnt vmcnt(0)\n"
  "L12_%=:\n"
  "v_xor_b32 v10, s8, v15\n"
  "v_xor_b32 v11, s8, v17\n"
  "v_bitop3_b32 v10, v10, v21, s8 bitop3:0xf6\n"
  "v_bitop3_b32 v11, v11, v23, s8 bitop3:0xf6\n"
  "v_bitop3_b32 v10, v10, v51, s8 bitop3:0xf6\n"
  "v_bitop3_b32 v11, v11, v53, s8 bitop3:0xf6\n"
  "v_bitop3_b32 v10, v10, v77, s8 bitop3:0xf6\n"
  "v_bitop3_b32 v11, v11, v79, s8 bitop3:0xf6\n"
  "v_bitop3_b32 v10, v10, v81, s8 bitop3:0xf6\n"
  "v_bitop3_b32 v11, v11, v83, s8 bitop3:0xf6\n"
  "v_bitop3_b32 v10, v10, v85, s8 bitop3:0xf6\n"
  "v_bitop3_b32 v11, v11, v87, s8 bitop3:0xf6\n"
  "v_bitop3_b32 v10, v10, v89, s8 bitop3:0xf6\n"
  "v_bitop3_b32 v11, v11, v91, s8 bitop3:0xf6\n"
  "v_bitop3_b32 v10, v10, v93, s8 bitop3:0xf6\n"
  "v_bitop3_b32 v11, v11, v95, s8 bitop3:0xf6\n"
  "v_or_b32 v10, v10, v11\n"
  "v_cmp_ne_u32 vcc, 0, v10\n"
  "s_nop 0\n"
  "s_and_b64 vcc, exec, vcc\n"
  "s_cbranch_vccnz L13_%=\n"
  "L14_%=:\n"
  "s_lshl_b32 s46, s10, 16\n"
  "s_and_b32 s47, s10, 0xffff0000\n"
  "v_and_b32 v81, 0xffff0000, v80\n"
  "v_and_b32 v83, 0xffff0000, v82\n"
  "v_rcp_f32 v81, v81\n"
  "v_rcp_f32 v83, v83\n"
  "v_lshlrev_b32 v80, 16, v80\n"
  "v_lshlrev_b32 v82, 16, v82\n"
  "v_lshlrev_b32 v24, 16, v14\n"
  "v_and_b32 v25, 0xffff0000, v14\n"
  "v_lshlrev_b32 v26, 16, v16\n"
  "v_and_b32 v27, 0xffff0000, v16\n"
  "ds_write_b128 v7, v[96:99] offset:8320\n"
  "v_pk_add_f32 v[14:15], v[24:25], v[80:81] op_sel_hi:[1,0] neg_lo:[0,1] neg_hi:[0,1]\n"
  "v_pk_add_f32 v[16:17], v[26:27], v[82:83] op_sel_hi:[1,0] neg_lo:[0,1] neg_hi:[0,1]\n"
  "v_pk_mul_f32 v[14:15], v[14:15], v[80:81] op_sel:[0,1]\n"
  "v_pk_mul_f32 v[16:17], v[16:17], v[82:83] op_sel:[0,1]\n"
  "v_cvt_pk_f16_f32 v80, v16, v14\n"
  "v_cvt_pk_f16_f32 v82, v17, v15\n"
  "v_pk_fma_f32 v[24:25], v[14:15], s[46:47], v[24:25]\n"
  "v_pk_fma_f32 v[26:27], v[16:17], s[46:47], v[26:27]\n"
  "ds_write_b128 v7, v[100:103] offset:12480\n"
  "v_and_b32 v85, 0xffff0000, v84\n"
  "v_and_b32 v87, 0xffff0000, v86\n"
  "v_rcp_f32 v85, v85\n"
  "v_rcp_f32 v87, v87\n"
  "v_lshlrev_b32 v84, 16, v84\n"
  "v_lshlrev_b32 v86, 16, v86\n"
  "v_lshlrev_b32 v28, 16, v20\n"
  "v_and_b32 v29, 0xffff0000, v20\n"
  "v_lshlrev_b32 v30, 16, v22\n"
  "v_and_b32 v31, 0xffff0000, v22\n"
  "ds_write_b128 v13, v[104:107] offset:0\n"
  "v_pk_add_f32 v[20:21], v[28:29], v[84:85] op_sel_hi:[1,0] neg_lo:[0,1] neg_hi:[0,1]\n"
  "v_pk_add_f32 v[22:23], v[30:31], v[86:87] op_sel_hi:[1,0] neg_lo:[0,1] neg_hi:[0,1]\n"
  "v_pk_mul_f32 v[20:21], v[20:21], v[84:85] op_sel:[0,1]\n"
  "v_pk_mul_f32 v[22:23], v[22:23], v[86:87] op_sel:[0,1]\n"
  "v_cvt_pk_f16_f32 v84, v22, v20\n"
  "v_cvt_pk_f16_f32 v86, v23, v21\n"
  "v_pk_fma_f32 v[28:29], v[20:21], s[46:47], v[28:29]\n"
  "v_pk_fma_f32 v[30:31], v[22:23], s[46:47], v[30:31]\n"
  "ds_write_b128 v13, v[108:111] offset:4160\n"
  "v_and_b32 v89, 0xffff0000, v88\n"
  "v_and_b32 v91, 0xffff0000, v90\n"
  "v_rcp_f32 v89, v89\n"
  "v_rcp_f32 v91, v91\n"
  "v_lshlrev_b32 v88, 16, v88\n"
  "v_lshlrev_b32 v90, 16, v90\n"
  "v_lshlrev_b32 v58, 16, v50\n"
  "v_and_b32 v59, 0xffff0000, v50\n"
  "v_lshlrev_b32 v60, 16, v52\n"
  "v_and_b32 v61, 0xffff0000, v52\n"
  "ds_write_b128 v13, v[112:115] offset:8320\n"
  "v_pk_add_f32 v[50:51], v[58:59], v[88:89] op_sel_hi:[1,0] neg_lo:[0,1] neg_hi:[0,1]\n"
  "v_pk_add_f32 v[52:53], v[60:61], v[90:91] op_sel_hi:[1,0] neg_lo:[0,1] neg_hi:[0,1]\n"
  "v_pk_mul_f32 v[50:51], v[50:51], v[88:89] op_sel:[0,1]\n"
  "v_pk_mul_f32 v[52:53], v[52:53], v[90:91] op_sel:[0,1]\n"
  "v_cvt_pk_f16_f32 v88, v52, v50\n"
  "v_cvt_pk_f16_f32 v90, v53, v51\n"
  "v_pk_fma_f32 v[58:59], v[50:51], s[46:47], v[58:59]\n"
  "v_pk_fma_f32 v[60:61], v[52:53], s[46:47], v[60:61]\n"
  "ds_write_b128 v13, v[116:119] offset:12480\n"
  "v_and_b32 v93, 0xffff0000, v92\n"
  "v_and_b32 v95, 0xffff0000, v94\n"
  "v_rcp_f32 v93, v93\n"
  "v_rcp_f32 v95, v95\n"
  "v_lshlrev_b32 v92, 16, v92\n"
  "v_lshlrev_b32 v94, 16, v94\n"
  "v_lshlrev_b32 v62, 16, v76\n"
  "v_and_b32 v63, 0xffff0000, v76\n"
  "v_lshlrev_b32 v64, 16, v78\n"
  "v_and_b32 v65, 0xffff0000, v78\n"
  "v_pk_add_f32 v[76:77], v[62:63], v[92:93] op_sel_hi:[1,0] neg_lo:[0,1] neg_hi:[0,1]\n"
  "v_pk_add_f32 v[78:79], v[64:65], v[94:95] op_sel_hi:[1,0] neg_lo:[0,1] neg_hi:[0,1]\n"
  "v_pk_mul_f32 v[76:77], v[76:77], v[92:93] op_sel:[0,1]\n"
  "v_pk_mul_f32 v[78:79], v[78:79], v[94:95] op_sel:[0,1]\n"
  "v_cvt_pk_f16_f32 v92, v78, v76\n"
  "v_cvt_pk_f16_f32 v94, v79, v77\n"
  "v_pk_fma_f32 v[62:63], v[76:77], s[46:47], v[62:63]\n"
  "v_pk_fma_f32 v[64:65], v[78:79], s[46:47], v[64:65]\n"
  "ds_write_b32 v40, v80 offset:34720\n"
  "ds_write_b32 v40, v82 offset:42304\n"
  "ds_write_b32 v40, v84 offset:36256\n"
  "ds_write_b32 v40, v86 offset:43840\n"
  "ds_write_b32 v40, v88 offset:37792\n"
  "ds_write_b32 v40, v90 offset:45376\n"
  "ds_write_b32 v40, v92 offset:39328\n"
  "ds_write_b32 v40, v94 offset:46912\n"
  "s_waitcnt lgkmcnt(0)\n"
  "s_barrier\n"
  "s_cmp_eq_u32 s62, 0\n"
  "s_cbranch_scc1 L15_%=\n"
  "L16_%=:\n"
  "v_mov_b32 v10, 0\n"
  "ds_read_b64 v[6:7], v10 offset:48448\n"
  "s_mul_i32 s22, s3, 5120\n"
  "ds_read_b64 a[0:1], v41 offset:0\n"
  "ds_read_b64 a[2:3], v41 offset:8\n"
  "ds_read_b128 a[4:7], v42 offset:672\n"
  "ds_read_b128 a[8:11], v42 offset:2208\n"
  "ds_read_b64 a[12:13], v41 offset:32\n"
  "ds_read_b64 a[14:15], v41 offset:40\n"
  "ds_read_b64 a[16:17], v41 offset:1024\n"
  "ds_read_b64 a[18:19], v41 offset:1032\n"
  "ds_read_b64 a[20:21], v41 offset:1056\n"
  "ds_read_b64 a[22:23], v41 offset:1064\n"
  "ds_read_b64 a[24:25], v41 offset:64\n"
  "ds_read_b64 a[26:27], v41 offset:72\n"
  "ds_read_b128 a[28:31], v42 offset:576\n"
  "ds_read_b128 a[32:35], v42 offset:2112\n"
  "s_waitcnt lgkmcnt(6)\n"
  "v_mfma_f32_16x16x32_f16 v[14:17], a[4:7], a[0:3], 0\n"
  "ds_read_b64 a[36:37], v41 offset:96\n"
  "ds_read_b64 a[38:39], v41 offset:104\n"
  "v_mfma_f32_16x16x32_f16 v[32:35], a[8:11], a[0:3], 0\n"
  "ds_read_b64 a[40:41], v41 offset:1088\n"
  "ds_read_b64 a[42:43], v41 offset:1096\n"
  "v_mfma_f32_16x16x32_f16 v[18:21], a[4:7], a[12:15], 0\n"
  "ds_read_b64 a[44:45], v41 offset:1120\n"
  "ds_read_b64 a[46:47], v41 offset:1128\n"
  "v_mfma_f32_16x16x32_f16 v[50:53], a[8:11], a[12:15], 0\n"
  "ds_read_b64 a[48:49], v41 offset:128\n"
  "ds_read_b64 a[50:51], v41 offset:136\n"
  "v_mfma_f32_16x16x32_f16 v[32:35], a[4:7], a[16:19], v[32:35]\n"
  "ds_read_b128 a[52:55], v42 offset:480\n"
  "s_waitcnt lgkmcnt(7)\n"
  "v_mfma_f32_16x16x32_f16 v[50:53], a[4:7], a[20:23], v[50:53]\n"
  "ds_read_b128 a[56:59], v42 offset:2016\n"
  "ds_read_b64 a[60:61], v41 offset:160\n"
  "s_add_u32 s39, s40, 172032\n"
  "s_add_u32 m0, s37, 73728\n"
  "v_mfma_f32_16x16x32_f16 v[14:17], a[28:31], a[24:27], v[14:17]\n"
  "buffer_load_dwordx4 v43, s[28:31], s39 offen lds\n"
  "ds_read_b64 a[62:63], v41 offset:168\n"
  "ds_read_b64 a[64:65], v41 offset:1152\n"
  "v_mfma_f32_16x16x32_f16 v[32:35], a[32:35], a[24:27], v[32:35]\n"
  "ds_read_b64 a[66:67], v41 offset:1160\n"
  "ds_read_b64 a[68:69], v41 offset:1184\n"
  "v_mfma_f32_16x16x32_f16 v[18:21], a[28:31], a[36:39], v[18:21]\n"
  "ds_read_b64 a[70:71], v41 offset:1192\n"
  "ds_read_b64 a[72:73], v41 offset:192\n"
  "v_mfma_f32_16x16x32_f16 v[50:53], a[32:35], a[36:39], v[50:53]\n"
  "s_waitcnt lgkmcnt(7)\n"
  "v_mfma_f32_16x16x32_f16 v[32:35], a[28:31], a[40:43], v[32:35]\n"
  "ds_read_b64 a[74:75], v41 offset:200\n"
  "ds_read_b128 a[76:79], v42 offset:384\n"
  "s_add_u32 m0, s37, 77824\n"
  "v_mfma_f32_16x16x32_f16 v[50:53], a[28:31], a[44:47], v[50:53]\n"
  "buffer_load_dwordx4 v43, s[32:35], s39 offen lds\n"
  "ds_read_b128 a[80:83], v42 offset:1920\n"
  "ds_read_b64 a[84:85], v41 offset:224\n"
  "v_mfma_f32_16x16x32_f16 v[14:17], a[52:55], a[48:51], v[14:17]\n"
  "ds_read_b64 a[86:87], v41 offset:232\n"
  "ds_read_b64 a[88:89], v41 offset:1216\n"
  "v_mfma_f32_16x16x32_f16 v[32:35], a[56:59], a[48:51], v[32:35]\n"
  "ds_read_b64 a[90:91], v41 offset:1224\n"
  "ds_read_b64 a[92:93], v41 offset:1248\n"
  "s_waitcnt lgkmcnt(6)\n"
  "v_mfma_f32_16x16x32_f16 v[18:21], a[52:55], a[60:63], v[18:21]\n"
  "ds_read_b64 a[94:95], v41 offset:1256\n"
  "ds_read_b64 a[96:97], v41 offset:256\n"
  "v_mfma_f32_16x16x32_f16 v[50:53], a[56:59], a[60:63], v[50:53]\n"
  "ds_read_b64 a[98:99], v41 offset:264\n"
  "ds_read_b128 a[100:103], v42 offset:288\n"
  "s_add_u32 s39, s40, 229376\n"
  "s_add_u32 m0, s37, 81920\n"
  "v_mfma_f32_16x16x32_f16 v[32:35], a[52:55], a[64:67], v[32:35]\n"
  "buffer_load_dwordx4 v43, s[28:31], s39 offen lds\n"
  "ds_read_b128 a[104:107], v42 offset:1824\n"
  "ds_read_b64 a[108:109], v41 offset:288\n"
  "v_mfma_f32_16x16x32_f16 v[50:53], a[52:55], a[68:71], v[50:53]\n"
  "ds_read_b64 a[110:111], v41 offset:296\n"
  "v_mfma_f32_16x16x32_f16 v[14:17], a[76:79], a[72:75], v[14:17]\n"
  "ds_read_b64 a[112:113], v41 offset:1280\n"
  "ds_read_b64 a[114:115], v41 offset:1288\n"
  "s_waitcnt lgkmcnt(6)\n"
  "v_mfma_f32_16x16x32_f16 v[32:35], a[80:83], a[72:75], v[32:35]\n"
  "ds_read_b64 a[116:117], v41 offset:1312\n"
  "ds_read_b64 a[118:119], v41 offset:1320\n"
  "v_mfma_f32_16x16x32_f16 v[18:21], a[76:79], a[84:87], v[18:21]\n"
  "ds_read_b64 a[120:121], v41 offset:320\n"
  "ds_read_b64 a[122:123], v41 offset:328\n"
  "s_add_u32 m0, s37, 86016\n"
  "v_mfma_f32_16x16x32_f16 v[50:53], a[80:83], a[84:87], v[50:53]\n"
  "buffer_load_dwordx4 v43, s[32:35], s39 offen lds\n"
  "ds_read_b128 a[124:127], v42 offset:192\n"
  "ds_read_b128 a[128:131], v42 offset:1728\n"
  "v_mfma_f32_16x16x32_f16 v[32:35], a[76:79], a[88:91], v[32:35]\n"
  "ds_read_b64 a[132:133], v41 offset:352\n"
  "ds_read_b64 a[134:135], v41 offset:360\n"
  "v_mfma_f32_16x16x32_f16 v[50:53], a[76:79], a[92:95], v[50:53]\n"
  "s_waitcnt lgkmcnt(6)\n"
  "v_mfma_f32_16x16x32_f16 v[14:17], a[100:103], a[96:99], v[14:17]\n"
  "ds_read_b64 a[136:137], v41 offset:1344\n"
  "ds_read_b64 a[138:139], v41 offset:1352\n"
  "v_mfma_f32_16x16x32_f16 v[32:35], a[104:107], a[96:99], v[32:35]\n"
  "ds_read_b64 a[140:141], v41 offset:1376\n"
  "ds_read_b64 a[142:143], v41 offset:1384\n"
  "s_add_u32 s39, s40, 286720\n"
  "s_add_u32 m0, s37, 90112\n"
  "v_mfma_f32_16x16x32_f16 v[18:21], a[100:103], a[108:111], v[18:21]\n"
  "buffer_load_dwordx4 v43, s[28:31], s39 offen lds\n"
  "ds_read_b64 a[144:145], v41 offset:384\n"
  "ds_read_b64 a[146:147], v41 offset:392\n"
  "v_mfma_f32_16x16x32_f16 v[50:53], a[104:107], a[108:111], v[50:53]\n"
  "ds_read_b128 a[148:151], v42 offset:96\n"
  "ds_read_b128 a[152:155], v42 offset:1632\n"
  "v_mfma_f32_16x16x32_f16 v[32:35], a[100:103], a[112:115], v[32:35]\n"
  "ds_read_b64 a[156:157], v41 offset:416\n"
  "v_mfma_f32_16x16x32_f16 v[50:53], a[100:103], a[116:119], v[50:53]\n"
  "s_waitcnt lgkmcnt(7)\n"
  "v_mfma_f32_16x16x32_f16 v[14:17], a[124:127], a[120:123], v[14:17]\n"
  "ds_read_b64 a[158:159], v41 offset:424\n"
  "ds_read_b64 a[160:161], v41 offset:1408\n"
  "s_add_u32 m0, s37, 94208\n"
  "v_mfma_f32_16x16x32_f16 v[32:35], a[128:131], a[120:123], v[32:35]\n"
  "buffer_load_dwordx4 v43, s[32:35], s39 offen lds\n"
  "ds_read_b64 a[162:163], v41 offset:1416\n"
  "ds_read_b64 a[164:165], v41 offset:1440\n"
  "v_mfma_f32_16x16x32_f16 v[18:21], a[124:127], a[132:135], v[18:21]\n"
  "ds_read_b64 a[166:167], v41 offset:1448\n"
  "ds_read_b64 a[168:169], v41 offset:448\n"
  "v_mfma_f32_16x16x32_f16 v[50:53], a[128:131], a[132:135], v[50:53]\n"
  "ds_read_b64 a[170:171], v41 offset:456\n"
  "ds_read_b128 a[172:175], v42 offset:0\n"
  "v_mfma_f32_16x16x32_f16 v[32:35], a[124:127], a[136:139], v[32:35]\n"
  "s_waitcnt lgkmcnt(7)\n"
  "v_mfma_f32_16x16x32_f16 v[50:53], a[124:127], a[140:143], v[50:53]\n"
  "ds_read_b128 a[176:179], v42 offset:1536\n"
  "ds_read_b64 a[180:181], v41 offset:480\n"
  "s_add_u32 s39, s40, 344064\n"
  "s_add_u32 m0, s37, 98304\n"
  "v_mfma_f32_16x16x32_f16 v[14:17], a[148:151], a[144:147], v[14:17]\n"
  "buffer_load_dwordx4 v43, s[28:31], s39 offen lds\n"
  "ds_read_b64 a[182:183], v41 offset:488\n"
  "ds_read_b64 a[184:185], v41 offset:1472\n"
  "v_mfma_f32_16x16x32_f16 v[32:35], a[152:155], a[144:147], v[32:35]\n"
  "ds_read_b64 a[186:187], v41 offset:1480\n"
  "ds_read_b64 a[188:189], v41 offset:1504\n"
  "v_mfma_f32_16x16x32_f16 v[18:21], a[148:151], a[156:159], v[18:21]\n"
  "ds_read_b64 a[190:191], v41 offset:1512\n"
  "v_mfma_f32_16x16x32_f16 v[50:53], a[152:155], a[156:159], v[50:53]\n"
  "ds_read_b128 v[78:81], v42 offset:3744\n"
  "s_waitcnt lgkmcnt(7)\n"
  "v_mfma_f32_16x16x32_f16 v[32:35], a[148:151], a[160:163], v[32:35]\n"
  "v_mfma_f32_16x16x32_f16 v[50:53], a[148:151], a[164:167], v[50:53]\n"
  "s_add_u32 m0, s37, 102400\n"
  "v_mfma_f32_16x16x32_f16 v[14:17], a[172:175], a[168:171], v[14:17]\n"
  "buffer_load_dwordx4 v43, s[32:35], s39 offen lds\n"
  "v_mfma_f32_16x16x32_f16 v[32:35], a[176:179], a[168:171], v[32:35]\n"
  "ds_read_b128 v[82:85], v42 offset:5280\n"
  "s_waitcnt lgkmcnt(6)\n"
  "v_mfma_f32_16x16x32_f16 v[18:21], a[172:175], a[180:183], v[18:21]\n"
  "v_mfma_f32_16x16x32_f16 v[50:53], a[176:179], a[180:183], v[50:53]\n"
  "s_waitcnt lgkmcnt(4)\n"
  "v_mfma_f32_16x16x32_f16 v[32:35], a[172:175], a[184:187], v[32:35]\n"
  "ds_read_b64 v[86:87], v41 offset:2048\n"
  "ds_read_b64 v[88:89], v41 offset:2056\n"
  "s_add_u32 s39, s40, 401408\n"
  "s_add_u32 m0, s37, 106496\n"
  "s_waitcnt lgkmcnt(4)\n"
  "v_mfma_f32_16x16x32_f16 v[50:53], a[172:175], a[188:191], v[50:53]\n"
  "buffer_load_dwordx4 v43, s[28:31], s39 offen lds\n"
  "v_mfma_f32_16x16x32_f16 v[54:57], a[8:11], a[16:19], 0\n"
  "ds_read_b64 v[90:91], v41 offset:2080\n"
  "ds_read_b64 v[92:93], v41 offset:2088\n"
  "s_waitcnt lgkmcnt(5)\n"
  "v_mfma_f32_16x16x32_f16 v[70:73], v[78:81], a[16:19], 0\n"
  "s_add_u32 m0, s22, 49152\n"
  "s_nop 0\n"
  "ds_write_addtid_b32 v14 offset:0\n"
  "ds_write_addtid_b32 v18 offset:320\n"
  "ds_write_addtid_b32 v15 offset:640\n"
  "ds_write_addtid_b32 v19 offset:960\n"
  "ds_write_addtid_b32 v16 offset:1280\n"
  "ds_write_addtid_b32 v20 offset:1600\n"
  "ds_write_addtid_b32 v17 offset:1920\n"
  "ds_write_addtid_b32 v21 offset:2240\n"
  "ds_write_addtid_b32 v32 offset:2560\n"
  "ds_write_addtid_b32 v50 offset:2880\n"
  "ds_write_addtid_b32 v33 offset:3200\n"
  "ds_write_addtid_b32 v51 offset:3520\n"
  "ds_write_addtid_b32 v34 offset:3840\n"
  "ds_write_addtid_b32 v52 offset:4160\n"
  "ds_write_addtid_b32 v35 offset:4480\n"
  "ds_write_addtid_b32 v53 offset:4800\n"
  "v_mfma_f32_16x16x32_f16 v[66:69], a[8:11], a[20:23], 0\n"
  "v_mfma_f32_16x16x32_f16 v[74:77], v[78:81], a[20:23], 0\n"
  "s_add_u32 m0, s37, 110592\n"
  "v_mfma_f32_16x16x32_f16 v[54:57], v[78:81], a[0:3], v[54:57]\n"
  "buffer_load_dwordx4 v43, s[32:35], s39 offen lds\n"
  "s_waitcnt lgkmcnt(15)\n"
  "v_mfma_f32_16x16x32_f16 v[70:73], v[82:85], a[0:3], v[70:73]\n"
  "s_waitcnt lgkmcnt(0)\n"
  "s_barrier\n"
  "ds_read_b64 v[14:15], v46 offset:20480\n"
  "ds_read_b64 v[16:17], v46 offset:25600\n"
  "ds_read_b64 v[18:19], v46 offset:30720\n"
  "ds_read_b64 v[20:21], v46 offset:35840\n"
  "ds_read_b64 v[32:33], v46 offset:23040\n"
  "ds_read_b64 v[34:35], v46 offset:28160\n"
  "ds_read_b64 v[50:51], v46 offset:33280\n"
  "ds_read_b64 v[52:53], v46 offset:38400\n"
  "v_mfma_f32_16x16x32_f16 v[66:69], v[78:81], a[12:15], v[66:69]\n"
  "v_mfma_f32_16x16x32_f16 v[74:77], v[82:85], a[12:15], v[74:77]\n"
  "v_mfma_f32_16x16x32_f16 v[54:57], a[4:7], v[86:89], v[54:57]\n"
  "s_add_u32 s39, s40, 458752\n"
  "s_add_u32 m0, s37, 114688\n"
  "v_mfma_f32_16x16x32_f16 v[70:73], a[8:11], v[86:89], v[70:73]\n"
  "buffer_load_dwordx4 v43, s[28:31], s39 offen lds\n"
  "v_mfma_f32_16x16x32_f16 v[66:69], a[4:7], v[90:93], v[66:69]\n"
  "v_mfma_f32_16x16x32_f16 v[74:77], a[8:11], v[90:93], v[74:77]\n"
  "ds_read_b64 v[94:95], v41 offset:3072\n"
  "ds_read_b64 v[96:97], v41 offset:3080\n"
  "s_waitcnt lgkmcnt(0)\n"
  "v_mfma_f32_16x16x32_f16 v[70:73], a[4:7], v[94:97], v[70:73]\n"
  "ds_read_b64 v[98:99], v41 offset:3104\n"
  "ds_read_b64 v[100:101], v41 offset:3112\n"
  "s_waitcnt lgkmcnt(0)\n"
  "v_mfma_f32_16x16x32_f16 v[74:77], a[4:7], v[98:101], v[74:77]\n"
  "ds_read_b128 v[102:105], v42 offset:3648\n"
  "ds_read_b128 v[106:109], v42 offset:5184\n"
  "s_add_u32 m0, s37, 118784\n"
  "v_mfma_f32_16x16x32_f16 v[54:57], a[32:35], a[40:43], v[54:57]\n"
  "buffer_load_dwordx4 v43, s[32:35], s39 offen lds\n"
  "ds_read_b64 v[110:111], v41 offset:2112\n"
  "s_waitcnt lgkmcnt(7)\n"
  "v_pk_add_f32 v[14:15], v[14:15], v[16:17]\n"
  "v_pk_add_f32 v[18:19], v[18:19], v[20:21]\n"
  "v_fmac_f32 v24, v14, v6\n"
  "v_fmac_f32 v25, v18, v7\n"
  "v_fmac_f32 v26, v15, v6\n"
  "v_fmac_f32 v27, v19, v7\n"
  "v_cvt_pk_bf16_f32 v24, v24, v25\n"
  "v_cvt_pk_bf16_f32 v25, v26, v27\n"
  "v_pk_add_f32 v[32:33], v[32:33], v[34:35]\n"
  "v_pk_add_f32 v[50:51], v[50:51], v[52:53]\n"
  "v_fmac_f32 v28, v32, v6\n"
  "v_fmac_f32 v29, v50, v7\n"
  "v_fmac_f32 v30, v33, v6\n"
  "v_fmac_f32 v31, v51, v7\n"
  "v_cvt_pk_bf16_f32 v28, v28, v29\n"
  "v_cvt_pk_bf16_f32 v29, v30, v31\n"
  "buffer_store_dwordx2 v[24:25], v47, s[24:27], 0 offen offset:0 sc1\n"
  "buffer_store_dwordx2 v[28:29], v47, s[24:27], 0 offen offset:2048 sc1\n"
  "v_add_u32 v47, 4096, v47\n"
  "s_waitcnt lgkmcnt(2)\n"
  "v_mfma_f32_16x16x32_f16 v[70:73], v[102:105], a[40:43], v[70:73]\n"
  "ds_read_b64 v[112:113], v41 offset:2120\n"
  "ds_read_b64 v[114:115], v41 offset:2144\n"
  "v_mfma_f32_16x16x32_f16 v[66:69], a[32:35], a[44:47], v[66:69]\n"
  "ds_read_b64 v[116:117], v41 offset:2152\n"
  "ds_read_b64 v[118:119], v41 offset:3136\n"
  "v_mfma_f32_16x16x32_f16 v[74:77], v[102:105], a[44:47], v[74:77]\n"
  "ds_read_b64 v[120:121], v41 offset:3144\n"
  "ds_read_b64 v[122:123], v41 offset:3168\n"
  "v_mfma_f32_16x16x32_f16 v[54:57], v[102:105], a[24:27], v[54:57]\n"
  "ds_read_b64 v[124:125], v41 offset:3176\n"
  "s_add_u32 s39, s40, 516096\n"
  "s_add_u32 m0, s37, 122880\n"
  "s_waitcnt lgkmcnt(6)\n"
  "v_mfma_f32_16x16x32_f16 v[70:73], v[106:109], a[24:27], v[70:73]\n"
  "buffer_load_dwordx4 v43, s[28:31], s39 offen lds\n"
  "ds_read_b128 v[126:129], v42 offset:3552\n"
  "v_mfma_f32_16x16x32_f16 v[66:69], v[102:105], a[36:39], v[66:69]\n"
  "v_mfma_f32_16x16x32_f16 v[74:77], v[106:109], a[36:39], v[74:77]\n"
  "v_mfma_f32_16x16x32_f16 v[54:57], a[28:31], v[110:113], v[54:57]\n"
  "v_mfma_f32_16x16x32_f16 v[70:73], a[32:35], v[110:113], v[70:73]\n"
  "ds_read_b128 v[130:133], v42 offset:5088\n"
  "s_add_u32 m0, s37, 126976\n"
  "s_waitcnt lgkmcnt(6)\n"
  "v_mfma_f32_16x16x32_f16 v[66:69], a[28:31], v[114:117], v[66:69]\n"
  "buffer_load_dwordx4 v43, s[32:35], s39 offen lds\n"
  "v_mfma_f32_16x16x32_f16 v[74:77], a[32:35], v[114:117], v[74:77]\n"
  "s_waitcnt lgkmcnt(4)\n"
  "v_mfma_f32_16x16x32_f16 v[70:73], a[28:31], v[118:121], v[70:73]\n"
  "ds_read_b64 v[134:135], v41 offset:2176\n"
  "ds_read_b64 v[136:137], v41 offset:2184\n"
  "s_waitcnt lgkmcnt(4)\n"
  "v_mfma_f32_16x16x32_f16 v[74:77], a[28:31], v[122:125], v[74:77]\n"
  "v_mfma_f32_16x16x32_f16 v[54:57], a[56:59], a[64:67], v[54:57]\n"
  "ds_read_b64 v[138:139], v41 offset:2208\n"
  "ds_read_b64 v[140:141], v41 offset:2216\n"
  "s_waitcnt lgkmcnt(5)\n"
  "v_mfma_f32_16x16x32_f16 v[70:73], v[126:129], a[64:67], v[70:73]\n"
  "s_add_u32 s39, s40, 573440\n"
  "s_add_u32 m0, s37, 131072\n"
  "v_mfma_f32_16x16x32_f16 v[66:69], a[56:59], a[68:71], v[66:69]\n"
  "buffer_load_dwordx4 v43, s[28:31], s39 offen lds\n"
  "ds_read_b64 v[78:79], v41 offset:3200\n"
  "v_mfma_f32_16x16x32_f16 v[74:77], v[126:129], a[68:71], v[74:77]\n"
  "ds_read_b64 v[80:81], v41 offset:3208\n"
  "ds_read_b64 v[82:83], v41 offset:3232\n"
  "v_mfma_f32_16x16x32_f16 v[54:57], v[126:129], a[48:51], v[54:57]\n"
  "ds_read_b64 v[84:85], v41 offset:3240\n"
  "s_waitcnt lgkmcnt(6)\n"
  "v_mfma_f32_16x16x32_f16 v[70:73], v[130:133], a[48:51], v[70:73]\n"
  "ds_read_b128 v[86:89], v42 offset:3456\n"
  "v_mfma_f32_16x16x32_f16 v[66:69], v[126:129], a[60:63], v[66:69]\n"
  "s_add_u32 m0, s37, 135168\n"
  "v_mfma_f32_16x16x32_f16 v[74:77], v[130:133], a[60:63], v[74:77]\n"
  "buffer_load_dwordx4 v43, s[32:35], s39 offen lds\n"
  "v_mfma_f32_16x16x32_f16 v[54:57], a[52:55], v[134:137], v[54:57]\n"
  "v_mfma_f32_16x16x32_f16 v[70:73], a[56:59], v[134:137], v[70:73]\n"
  "ds_read_b128 v[90:93], v42 offset:4992\n"
  "s_waitcnt lgkmcnt(6)\n"
  "v_mfma_f32_16x16x32_f16 v[66:69], a[52:55], v[138:141], v[66:69]\n"
  "v_mfma_f32_16x16x32_f16 v[74:77], a[56:59], v[138:141], v[74:77]\n"
  "s_add_u32 s39, s40, 630784\n"
  "s_add_u32 m0, s37, 139264\n"
  "s_waitcnt lgkmcnt(4)\n"
  "v_mfma_f32_16x16x32_f16 v[70:73], a[52:55], v[78:81], v[70:73]\n"
  "buffer_load_dwordx4 v43, s[28:31], s39 offen lds\n"
  "ds_read_b64 v[94:95], v41 offset:2240\n"
  "s_waitcnt lgkmcnt(3)\n"
  "v_mfma_f32_16x16x32_f16 v[74:77], a[52:55], v[82:85], v[74:77]\n"
  "ds_read_b64 v[96:97], v41 offset:2248\n"
  "v_mfma_f32_16x16x32_f16 v[54:57], a[80:83], a[88:91], v[54:57]\n"
  "ds_read_b64 v[98:99], v41 offset:2272\n"
  "ds_read_b64 v[100:101], v41 offset:2280\n"
  "s_waitcnt lgkmcnt(5)\n"
  "v_mfma_f32_16x16x32_f16 v[70:73], v[86:89], a[88:91], v[70:73]\n"
  "v_mfma_f32_16x16x32_f16 v[66:69], a[80:83], a[92:95], v[66:69]\n"
  "ds_read_b64 v[102:103], v41 offset:3264\n"
  "ds_read_b64 v[104:105], v41 offset:3272\n"
  "s_add_u32 m0, s37, 143360\n"
  "v_mfma_f32_16x16x32_f16 v[74:77], v[86:89], a[92:95], v[74:77]\n"
  "buffer_load_dwordx4 v43, s[32:35], s39 offen lds\n"
  "ds_read_b64 v[106:107], v41 offset:3296\n"
  "v_mfma_f32_16x16x32_f16 v[54:57], v[86:89], a[72:75], v[54:57]\n"
  "ds_read_b64 v[108:109], v41 offset:3304\n"
  "s_waitcnt lgkmcnt(6)\n"
  "v_mfma_f32_16x16x32_f16 v[70:73], v[90:93], a[72:75], v[70:73]\n"
  "ds_read_b128 v[110:113], v42 offset:3360\n"
  "v_mfma_f32_16x16x32_f16 v[66:69], v[86:89], a[84:87], v[66:69]\n"
  "v_mfma_f32_16x16x32_f16 v[74:77], v[90:93], a[84:87], v[74:77]\n"
  "s_add_u32 s39, s40, 688128\n"
  "s_add_u32 m0, s37, 147456\n"
  "v_mfma_f32_16x16x32_f16 v[54:57], a[76:79], v[94:97], v[54:57]\n"
  "buffer_load_dwordx4 v43, s[28:31], s39 offen lds\n"
  "v_mfma_f32_16x16x32_f16 v[70:73], a[80:83], v[94:97], v[70:73]\n"
  "ds_read_b128 v[114:117], v42 offset:4896\n"
  "s_waitcnt lgkmcnt(6)\n"
  "v_mfma_f32_16x16x32_f16 v[66:69], a[76:79], v[98:101], v[66:69]\n"
  "v_mfma_f32_16x16x32_f16 v[74:77], a[80:83], v[98:101], v[74:77]\n"
  "s_waitcnt lgkmcnt(4)\n"
  "v_mfma_f32_16x16x32_f16 v[70:73], a[76:79], v[102:105], v[70:73]\n"
  "ds_read_b64 v[118:119], v41 offset:2304\n"
  "ds_read_b64 v[120:121], v41 offset:2312\n"
  "s_add_u32 m0, s37, 151552\n"
  "s_waitcnt lgkmcnt(4)\n"
  "v_mfma_f32_16x16x32_f16 v[74:77], a[76:79], v[106:109], v[74:77]\n"
  "buffer_load_dwordx4 v43, s[32:35], s39 offen lds\n"
  "v_mfma_f32_16x16x32_f16 v[54:57], a[104:107], a[112:115], v[54:57]\n"
  "ds_read_b64 v[122:123], v41 offset:2336\n"
  "ds_read_b64 v[124:125], v41 offset:2344\n"
  "s_waitcnt lgkmcnt(5)\n"
  "v_mfma_f32_16x16x32_f16 v[70:73], v[110:113], a[112:115], v[70:73]\n"
  "v_mfma_f32_16x16x32_f16 v[66:69], a[104:107], a[116:119], v[66:69]\n"
  "ds_read_b64 v[126:127], v41 offset:3328\n"
  "ds_read_b64 v[128:129], v41 offset:3336\n"
  "v_mfma_f32_16x16x32_f16 v[74:77], v[110:113], a[116:119], v[74:77]\n"
  "ds_read_b64 v[130:131], v41 offset:3360\n"
  "ds_read_b64 v[132:133], v41 offset:3368\n"
  "s_add_u32 s39, s40, 745472\n"
  "s_add_u32 m0, s37, 155648\n"
  "v_mfma_f32_16x16x32_f16 v[54:57], v[110:113], a[96:99], v[54:57]\n"
  "buffer_load_dwordx4 v43, s[28:31], s39 offen lds\n"
  "s_waitcnt lgkmcnt(6)\n"
  "v_mfma_f32_16x16x32_f16 v[70:73], v[114:117], a[96:99], v[70:73]\n"
  "ds_read_b128 v[134:137], v42 offset:3264\n"
  "v_mfma_f32_16x16x32_f16 v[66:69], v[110:113], a[108:111], v[66:69]\n"
  "v_mfma_f32_16x16x32_f16 v[74:77], v[114:117], a[108:111], v[74:77]\n"
  "v_mfma_f32_16x16x32_f16 v[54:57], a[100:103], v[118:121], v[54:57]\n"
  "v_mfma_f32_16x16x32_f16 v[70:73], a[104:107], v[118:121], v[70:73]\n"
  "ds_read_b128 v[138:141], v42 offset:4800\n"
  "s_add_u32 m0, s37, 159744\n"
  "s_waitcnt lgkmcnt(6)\n"
  "v_mfma_f32_16x16x32_f16 v[66:69], a[100:103], v[122:125], v[66:69]\n"
  "buffer_load_dwordx4 v43, s[32:35], s39 offen lds\n"
  "v_mfma_f32_16x16x32_f16 v[74:77], a[104:107], v[122:125], v[74:77]\n"
  "s_waitcnt lgkmcnt(4)\n"
  "v_mfma_f32_16x16x32_f16 v[70:73], a[100:103], v[126:129], v[70:73]\n"
  "ds_read_b64 v[78:79], v41 offset:2368\n"
  "ds_read_b64 v[80:81], v41 offset:2376\n"
  "s_waitcnt vmcnt(10)\n"
  "s_barrier\n"
  "s_and_b64 exec, s[44:45], 0xff\n"
  "buffer_atomic_add v48, v44, s[24:27], 0 offen offset:0\n"
  "s_mov_b64 exec, -1\n"
  "s_add_u32 s39, s40, 0\n"
  "s_add_u32 m0, s37, 49152\n"
  "s_waitcnt lgkmcnt(4)\n"
  "v_mfma_f32_16x16x32_f16 v[74:77], a[100:103], v[130:133], v[74:77]\n"
  "buffer_load_dwordx4 v43, s[28:31], s39 offen lds\n"
  "v_mfma_f32_16x16x32_f16 v[54:57], a[128:131], a[136:139], v[54:57]\n"
  "ds_read_b64 v[82:83], v41 offset:2400\n"
  "ds_read_b64 v[84:85], v41 offset:2408\n"
  "s_waitcnt lgkmcnt(5)\n"
  "v_mfma_f32_16x16x32_f16 v[70:73], v[134:137], a[136:139], v[70:73]\n"
  "v_mfma_f32_16x16x32_f16 v[66:69], a[128:131], a[140:143], v[66:69]\n"
  "ds_read_b64 v[86:87], v41 offset:3392\n"
  "ds_read_b64 v[88:89], v41 offset:3400\n"
  "v_mfma_f32_16x16x32_f16 v[74:77], v[134:137], a[140:143], v[74:77]\n"
  "ds_read_b64 v[90:91], v41 offset:3424\n"
  "ds_read_b64 v[92:93], v41 offset:3432\n"
  "s_add_u32 m0, s37, 53248\n"
  "v_mfma_f32_16x16x32_f16 v[54:57], v[134:137], a[120:123], v[54:57]\n"
  "buffer_load_dwordx4 v43, s[32:35], s39 offen lds\n"
  "s_waitcnt lgkmcnt(6)\n"
  "v_mfma_f32_16x16x32_f16 v[70:73], v[138:141], a[120:123], v[70:73]\n"
  "ds_read_b128 v[94:97], v42 offset:3168\n"
  "v_mfma_f32_16x16x32_f16 v[66:69], v[134:137], a[132:135], v[66:69]\n"
  "v_mfma_f32_16x16x32_f16 v[74:77], v[138:141], a[132:135], v[74:77]\n"
  "v_mfma_f32_16x16x32_f16 v[54:57], a[124:127], v[78:81], v[54:57]\n"
  "v_mfma_f32_16x16x32_f16 v[70:73], a[128:131], v[78:81], v[70:73]\n"
  "ds_read_b128 v[98:101], v42 offset:4704\n"
  "s_add_u32 s39, s40, 57344\n"
  "s_add_u32 m0, s37, 57344\n"
  "s_waitcnt lgkmcnt(6)\n"
  "v_mfma_f32_16x16x32_f16 v[66:69], a[124:127], v[82:85], v[66:69]\n"
  "buffer_load_dwordx4 v43, s[28:31], s39 offen lds\n"
  "v_mfma_f32_16x16x32_f16 v[74:77], a[128:131], v[82:85], v[74:77]\n"
  "s_waitcnt lgkmcnt(4)\n"
  "v_mfma_f32_16x16x32_f16 v[70:73], a[124:127], v[86:89], v[70:73]\n"
  "ds_read_b64 v[102:103], v41 offset:2432\n"
  "ds_read_b64 v[104:105], v41 offset:2440\n"
  "s_waitcnt lgkmcnt(4)\n"
  "v_mfma_f32_16x16x32_f16 v[74:77], a[124:127], v[90:93], v[74:77]\n"
  "v_mfma_f32_16x16x32_f16 v[54:57], a[152:155], a[160:163], v[54:57]\n"
  "ds_read_b64 v[106:107], v41 offset:2464\n"
  "ds_read_b64 v[108:109], v41 offset:2472\n"
  "s_waitcnt lgkmcnt(5)\n"
  "v_mfma_f32_16x16x32_f16 v[70:73], v[94:97], a[160:163], v[70:73]\n"
  "s_add_u32 m0, s37, 61440\n"
  "v_mfma_f32_16x16x32_f16 v[66:69], a[152:155], a[164:167], v[66:69]\n"
  "buffer_load_dwordx4 v43, s[32:35], s39 offen lds\n"
  "ds_read_b64 v[110:111], v41 offset:3456\n"
  "v_mfma_f32_16x16x32_f16 v[74:77], v[94:97], a[164:167], v[74:77]\n"
  "ds_read_b64 v[112:113], v41 offset:3464\n"
  "ds_read_b64 v[114:115], v41 offset:3488\n"
  "v_mfma_f32_16x16x32_f16 v[54:57], v[94:97], a[144:147], v[54:57]\n"
  "ds_read_b64 v[116:117], v41 offset:3496\n"
  "s_waitcnt lgkmcnt(6)\n"
  "v_mfma_f32_16x16x32_f16 v[70:73], v[98:101], a[144:147], v[70:73]\n"
  "ds_read_b128 v[118:121], v42 offset:3072\n"
  "v_mfma_f32_16x16x32_f16 v[66:69], v[94:97], a[156:159], v[66:69]\n"
  "v_mfma_f32_16x16x32_f16 v[74:77], v[98:101], a[156:159], v[74:77]\n"
  "s_add_u32 s39, s40, 114688\n"
  "s_add_u32 m0, s37, 65536\n"
  "v_mfma_f32_16x16x32_f16 v[54:57], a[148:151], v[102:105], v[54:57]\n"
  "buffer_load_dwordx4 v43, s[28:31], s39 offen lds\n"
  "v_mfma_f32_16x16x32_f16 v[70:73], a[152:155], v[102:105], v[70:73]\n"
  "ds_read_b128 v[122:125], v42 offset:4608\n"
  "s_waitcnt lgkmcnt(6)\n"
  "v_mfma_f32_16x16x32_f16 v[66:69], a[148:151], v[106:109], v[66:69]\n"
  "v_mfma_f32_16x16x32_f16 v[74:77], a[152:155], v[106:109], v[74:77]\n"
  "s_waitcnt lgkmcnt(4)\n"
  "v_mfma_f32_16x16x32_f16 v[70:73], a[148:151], v[110:113], v[70:73]\n"
  "ds_read_b64 v[126:127], v41 offset:2496\n"
  "ds_read_b64 v[128:129], v41 offset:2504\n"
  "s_waitcnt lgkmcnt(4)\n"
  "v_mfma_f32_16x16x32_f16 v[74:77], a[148:151], v[114:117], v[74:77]\n"
  "s_add_u32 m0, s37, 69632\n"
  "v_mfma_f32_16x16x32_f16 v[54:57], a[176:179], a[184:187], v[54:57]\n"
  "buffer_load_dwordx4 v43, s[32:35], s39 offen lds\n"
  "ds_read_b64 v[130:131], v41 offset:2528\n"
  "s_waitcnt lgkmcnt(4)\n"
  "v_mfma_f32_16x16x32_f16 v[70:73], v[118:121], a[184:187], v[70:73]\n"
  "ds_read_b64 v[132:133], v41 offset:2536\n"
  "v_mfma_f32_16x16x32_f16 v[66:69], a[176:179], a[188:191], v[66:69]\n"
  "ds_read_b64 v[134:135], v41 offset:3520\n"
  "ds_read_b64 v[136:137], v41 offset:3528\n"
  "v_mfma_f32_16x16x32_f16 v[74:77], v[118:121], a[188:191], v[74:77]\n"
  "ds_read_b64 v[138:139], v41 offset:3552\n"
  "ds_read_b64 v[140:141], v41 offset:3560\n"
  "v_mfma_f32_16x16x32_f16 v[54:57], v[118:121], a[168:171], v[54:57]\n"
  "s_waitcnt lgkmcnt(6)\n"
  "v_mfma_f32_16x16x32_f16 v[70:73], v[122:125], a[168:171], v[70:73]\n"
  "v_mfma_f32_16x16x32_f16 v[66:69], v[118:121], a[180:183], v[66:69]\n"
  "v_mfma_f32_16x16x32_f16 v[74:77], v[122:125], a[180:183], v[74:77]\n"
  "v_mfma_f32_16x16x32_f16 v[54:57], a[172:175], v[126:129], v[54:57]\n"
  "v_mfma_f32_16x16x32_f16 v[70:73], a[176:179], v[126:129], v[70:73]\n"
  "s_waitcnt lgkmcnt(4)\n"
  "v_mfma_f32_16x16x32_f16 v[66:69], a[172:175], v[130:133], v[66:69]\n"
  "v_mfma_f32_16x16x32_f16 v[74:77], a[176:179], v[130:133], v[74:77]\n"
  "s_waitcnt lgkmcnt(2)\n"
  "v_mfma_f32_16x16x32_f16 v[70:73], a[172:175], v[134:137], v[70:73]\n"
  "s_waitcnt lgkmcnt(0)\n"
  "v_mfma_f32_16x16x32_f16 v[74:77], a[172:175], v[138:141], v[74:77]\n"
  "s_add_u32 m0, s22, 28672\n"
  "s_barrier\n"
  "ds_write_addtid_b32 v54 offset:0\n"
  "ds_write_addtid_b32 v66 offset:320\n"
  "ds_write_addtid_b32 v55 offset:640\n"
  "ds_write_addtid_b32 v67 offset:960\n"
  "ds_write_addtid_b32 v56 offset:1280\n"
  "ds_write_addtid_b32 v68 offset:1600\n"
  "ds_write_addtid_b32 v57 offset:1920\n"
  "ds_write_addtid_b32 v69 offset:2240\n"
  "ds_write_addtid_b32 v70 offset:2560\n"
  "ds_write_addtid_b32 v74 offset:2880\n"
  "ds_write_addtid_b32 v71 offset:3200\n"
  "ds_write_addtid_b32 v75 offset:3520\n"
  "ds_write_addtid_b32 v72 offset:3840\n"
  "ds_write_addtid_b32 v76 offset:4160\n"
  "ds_write_addtid_b32 v73 offset:4480\n"
  "ds_write_addtid_b32 v77 offset:4800\n"
  "v_bfe_u32 v11, v2, 2, 2\n"
  "v_and_b32 v13, 1, v11\n"
  "v_lshrrev_b32 v22, 1, v11\n"
  "v_lshl_add_u32 v13, v22, 2, v13\n"
  "v_lshl_add_u32 v13, v4, 1, v13\n"
  "v_lshlrev_b32 v13, 8, v13\n"
  "v_xor_b32 v11, s3, v11\n"
  "v_lshl_add_u32 v13, v11, 6, v13\n"
  "v_and_b32 v11, 1, v37\n"
  "v_lshl_add_u32 v13, v11, 5, v13\n"
  "v_lshl_add_u32 v13, v8, 3, v13\n"
  "v_add_u32 v22, 106496, v13\n"
  "v_add_u32 v13, 49152, v13\n"
  "s_lshl_b32 s4, s18, 1\n"
  "s_add_u32 s4, s4, s3\n"
  "s_and_b32 s4, s4, 3\n"
  "s_mul_i32 s23, s4, 458752\n"
  "s_lshl_b32 s4, s16, 8\n"
  "s_add_u32 s4, s4, 5242880\n"
  "v_and_b32 v10, 7, v1\n"
  "v_lshrrev_b32 v11, 3, v2\n"
  "v_lshlrev_b32 v23, 13, v11\n"
  "v_lshl_add_u32 v23, v10, 4, v23\n"
  "v_add_u32 v23, s4, v23\n"
  "s_mul_i32 s43, s3, 7168\n"
  "v_lshlrev_b32 v38, 2, v3\n"
  "v_lshl_add_u32 v38, v4, 7, v38\n"
  "v_mov_b32 v5, 56\n"
  "s_waitcnt vmcnt(0)\n"
  "s_waitcnt lgkmcnt(0)\n"
  "s_barrier\n"
  "s_mov_b64 exec, s[44:45]\n"
  "buffer_load_dword v5, v45, s[24:27], 0 offen offset:0 sc1\n"
  "s_mov_b64 exec, -1\n"
  "ds_read_b64 v[54:55], v46 offset:0\n"
  "ds_read_b64 v[56:57], v46 offset:5120\n"
  "ds_read_b64 v[66:67], v46 offset:10240\n"
  "ds_read_b64 v[68:69], v46 offset:15360\n"
  "ds_read_b64 v[70:71], v46 offset:2560\n"
  "ds_read_b64 v[72:73], v46 offset:7680\n"
  "ds_read_b64 v[74:75], v46 offset:12800\n"
  "ds_read_b64 v[76:77], v46 offset:17920\n"
  "s_waitcnt lgkmcnt(4)\n"
  "v_pk_add_f32 v[54:55], v[54:55], v[56:57]\n"
  "v_pk_add_f32 v[66:67], v[66:67], v[68:69]\n"
  "v_fmac_f32 v58, v54, v6\n"
  "v_fmac_f32 v59, v66, v7\n"
  "v_fmac_f32 v60, v55, v6\n"
  "v_fmac_f32 v61, v67, v7\n"
  "v_cvt_pk_bf16_f32 v58, v58, v59\n"
  "v_cvt_pk_bf16_f32 v59, v60, v61\n"
  "buffer_store_dwordx2 v[58:59], v47, s[24:27], 0 offen offset:0 sc1\n"
  "s_waitcnt lgkmcnt(0)\n"
  "v_pk_add_f32 v[70:71], v[70:71], v[72:73]\n"
  "v_pk_add_f32 v[74:75], v[74:75], v[76:77]\n"
  "v_fmac_f32 v62, v70, v6\n"
  "v_fmac_f32 v63, v74, v7\n"
  "v_fmac_f32 v64, v71, v6\n"
  "v_fmac_f32 v65, v75, v7\n"
  "v_cvt_pk_bf16_f32 v62, v62, v63\n"
  "v_cvt_pk_bf16_f32 v63, v64, v65\n"
  "buffer_store_dwordx2 v[62:63], v47, s[24:27], 0 offen offset:2048 sc1\n"
  "ds_read_b64_tr_b16 a[0:1], v13 offset:0\n"
  "ds_read_b64_tr_b16 a[2:3], v13 offset:2048\n"
  "ds_read_b64_tr_b16 a[4:5], v13 offset:4096\n"
  "ds_read_b64_tr_b16 a[6:7], v13 offset:6144\n"
  "ds_read_b64_tr_b16 a[8:9], v13 offset:8192\n"
  "ds_read_b64_tr_b16 a[10:11], v13 offset:10240\n"
  "ds_read_b64_tr_b16 a[12:13], v13 offset:12288\n"
  "ds_read_b64_tr_b16 a[14:15], v13 offset:14336\n"
  "ds_read_b64_tr_b16 a[16:17], v13 offset:16384\n"
  "ds_read_b64_tr_b16 a[18:19], v13 offset:18432\n"
  "ds_read_b64_tr_b16 a[20:21], v13 offset:20480\n"
  "ds_read_b64_tr_b16 a[22:23], v13 offset:22528\n"
  "ds_read_b64_tr_b16 a[24:25], v13 offset:24576\n"
  "ds_read_b64_tr_b16 a[26:27], v13 offset:26624\n"
  "ds_read_b64_tr_b16 a[28:29], v13 offset:28672\n"
  "ds_read_b64_tr_b16 a[30:31], v13 offset:30720\n"
  "ds_read_b64_tr_b16 a[32:33], v13 offset:32768\n"
  "ds_read_b64_tr_b16 a[34:35], v13 offset:34816\n"
  "ds_read_b64_tr_b16 a[36:37], v13 offset:36864\n"
  "ds_read_b64_tr_b16 a[38:39], v13 offset:38912\n"
  "ds_read_b64_tr_b16 a[40:41], v13 offset:40960\n"
  "ds_read_b64_tr_b16 a[42:43], v13 offset:43008\n"
  "ds_read_b64_tr_b16 a[44:45], v13 offset:45056\n"
  "ds_read_b64_tr_b16 a[46:47], v13 offset:47104\n"
  "ds_read_b64_tr_b16 a[48:49], v13 offset:49152\n"
  "ds_read_b64_tr_b16 a[50:51], v13 offset:51200\n"
  "ds_read_b64_tr_b16 a[52:53], v13 offset:53248\n"
  "ds_read_b64_tr_b16 a[54:55], v13 offset:55296\n"
  "s_lshl_b32 s4, s17, 2\n"
  "s_add_u32 s4, s4, s3\n"
  "s_lshl_b32 s4, s4, 8\n"
  "s_add_u32 s4, s4, 196608\n"
  "v_lshl_add_u32 v6, v3, 3, s4\n"
  "s_lshl_b32 s4, s16, 9\n"
  "s_add_u32 s4, s4, 163840\n"
  "v_lshl_add_u32 v7, v3, 3, s4\n"
  "s_mov_b32 s46, 14336\n"
  "s_waitcnt vmcnt(2)\n"
  "L17_%=:\n"
  "v_cmp_gt_u32 vcc, 28, v5\n"
  "s_nop 0\n"
  "s_and_b64 vcc, exec, vcc\n"
  "s_cbranch_vccnz L18_%=\n"
  "L19_%=:\n"
  "s_waitcnt lgkmcnt(0)\n"
  "s_barrier\n"
  "s_add_u32 s39, s23, 0\n"
  "s_add_u32 m0, s43, 0\n"
  "ds_read_b64_tr_b16 a[56:57], v22 offset:0\n"
  "buffer_load_dwordx4 v23, s[24:27], s39 offen sc1 lds\n"
  "ds_read_b64_tr_b16 a[58:59], v22 offset:2048\n"
  "ds_read_b64_tr_b16 a[60:61], v22 offset:4096\n"
  "ds_read_b64_tr_b16 a[62:63], v22 offset:6144\n"
  "s_add_u32 s39, s23, 65536\n"
  "s_add_u32 m0, s43, 1024\n"
  "ds_read_b64_tr_b16 a[64:65], v22 offset:8192\n"
  "buffer_load_dwordx4 v23, s[24:27], s39 offen sc1 lds\n"
  "ds_read_b64_tr_b16 a[66:67], v22 offset:10240\n"
  "ds_read_b64_tr_b16 a[68:69], v22 offset:12288\n"
  "ds_read_b64_tr_b16 a[70:71], v22 offset:14336\n"
  "s_add_u32 s39, s23, 131072\n"
  "s_add_u32 m0, s43, 2048\n"
  "ds_read_b64_tr_b16 a[72:73], v22 offset:16384\n"
  "buffer_load_dwordx4 v23, s[24:27], s39 offen sc1 lds\n"
  "ds_read_b64_tr_b16 a[74:75], v22 offset:18432\n"
  "ds_read_b64_tr_b16 a[76:77], v22 offset:20480\n"
  "ds_read_b64_tr_b16 a[78:79], v22 offset:22528\n"
  "s_add_u32 s39, s23, 196608\n"
  "s_add_u32 m0, s43, 3072\n"
  "ds_read_b64_tr_b16 a[80:81], v22 offset:24576\n"
  "buffer_load_dwordx4 v23, s[24:27], s39 offen sc1 lds\n"
  "ds_read_b64_tr_b16 a[82:83], v22 offset:26624\n"
  "ds_read_b64_tr_b16 a[84:85], v22 offset:28672\n"
  "ds_read_b64_tr_b16 a[86:87], v22 offset:30720\n"
  "s_add_u32 s39, s23, 262144\n"
  "s_add_u32 m0, s43, 4096\n"
  "ds_read_b64_tr_b16 a[88:89], v22 offset:32768\n"
  "buffer_load_dwordx4 v23, s[24:27], s39 offen sc1 lds\n"
  "ds_read_b64_tr_b16 a[90:91], v22 offset:34816\n"
  "ds_read_b64_tr_b16 a[92:93], v22 offset:36864\n"
  "ds_read_b64_tr_b16 a[94:95], v22 offset:38912\n"
  "s_add_u32 s39, s23, 327680\n"
  "s_add_u32 m0, s43, 5120\n"
  "ds_read_b64_tr_b16 a[96:97], v22 offset:40960\n"
  "buffer_load_dwordx4 v23, s[24:27], s39 offen sc1 lds\n"
  "ds_read_b64_tr_b16 a[98:99], v22 offset:43008\n"
  "ds_read_b64_tr_b16 a[100:101], v22 offset:45056\n"
  "ds_read_b64_tr_b16 a[102:103], v22 offset:47104\n"
  "s_add_u32 s39, s23, 393216\n"
  "s_add_u32 m0, s43, 6144\n"
  "ds_read_b64_tr_b16 a[104:105], v22 offset:49152\n"
  "buffer_load_dwordx4 v23, s[24:27], s39 offen sc1 lds\n"
  "ds_read_b64_tr_b16 a[106:107], v22 offset:51200\n"
  "ds_read_b64_tr_b16 a[108:109], v22 offset:53248\n"
  "ds_read_b64_tr_b16 a[110:111], v22 offset:55296\n"
  "buffer_load_dwordx2 v[14:15], v6, s[24:27], 0 offen sc1\n"
  "buffer_load_dwordx2 v[16:17], v6, s[24:27], s46 offen sc1\n"
  "s_add_u32 s39, s41, 0\n"
  "s_add_u32 m0, s37, 49152\n"
  "s_nop 0\n"
  "buffer_load_dwordx4 v43, s[28:31], s39 offen lds\n"
  "s_add_u32 m0, s37, 53248\n"
  "s_nop 0\n"
  "buffer_load_dwordx4 v43, s[32:35], s39 offen lds\n"
  "s_add_u32 s39, s41, 57344\n"
  "s_add_u32 m0, s37, 57344\n"
  "s_nop 0\n"
  "buffer_load_dwordx4 v43, s[28:31], s39 offen lds\n"
  "s_add_u32 m0, s37, 61440\n"
  "s_nop 0\n"
  "buffer_load_dwordx4 v43, s[32:35], s39 offen lds\n"
  "s_add_u32 s39, s41, 114688\n"
  "s_add_u32 m0, s37, 65536\n"
  "s_nop 0\n"
  "buffer_load_dwordx4 v43, s[28:31], s39 offen lds\n"
  "s_add_u32 m0, s37, 69632\n"
  "s_nop 0\n"
  "buffer_load_dwordx4 v43, s[32:35], s39 offen lds\n"
  "s_add_u32 s39, s41, 172032\n"
  "s_add_u32 m0, s37, 73728\n"
  "s_nop 0\n"
  "buffer_load_dwordx4 v43, s[28:31], s39 offen lds\n"
  "s_add_u32 m0, s37, 77824\n"
  "s_nop 0\n"
  "buffer_load_dwordx4 v43, s[32:35], s39 offen lds\n"
  "s_waitcnt vmcnt(8)\n"
  "s_waitcnt lgkmcnt(0)\n"
  "s_barrier\n"
  "s_and_b64 exec, s[44:45], 0xff\n"
  "buffer_atomic_add v48, v44, s[24:27], 0 offen offset:128\n"
  "s_mov_b64 exec, -1\n"
  "ds_read2st64_b32 v[128:129], v38 offset0:0 offset1:1\n"
  "ds_read2st64_b32 v[130:131], v38 offset0:2 offset1:3\n"
  "ds_read2st64_b32 v[132:133], v38 offset0:4 offset1:5\n"
  "ds_read2st64_b32 v[134:135], v38 offset0:6 offset1:7\n"
  "L20_%=:\n"
  "v_xor_b32 v10, s8, v15\n"
  "v_bitop3_b32 v10, v10, v17, s8 bitop3:0xf6\n"
  "v_cmp_ne_u32 vcc, 0, v10\n"
  "s_nop 0\n"
  "s_and_b64 vcc, exec, vcc\n"
  "s_cbranch_vccnz L21_%=\n"
  "L22_%=:\n"
  "s_add_u32 s39, s41, 229376\n"
  "s_add_u32 m0, s37, 81920\n"
  "s_waitcnt lgkmcnt(2)\n"
  "v_mfma_f32_32x32x16_bf16 v[64:79], a[0:3], v[128:131], 0\n"
  "ds_read2st64_b32 v[136:137], v38 offset0:8 offset1:9\n"
  "ds_read2st64_b32 v[138:139], v38 offset0:10 offset1:11\n"
  "buffer_load_dwordx4 v43, s[28:31], s39 offen lds\n"
  "v_mfma_f32_32x32x16_bf16 v[96:111], a[4:7], v[128:131], 0\n"
  "v_sub_u32 v63, 1, v4\n"
  "v_cvt_f32_u32 v63, v63\n"
  "v_mul_f32 v14, v14, v63\n"
  "v_mov_b32 v20, 0\n"
  "s_add_u32 m0, s37, 86016\n"
  "s_waitcnt lgkmcnt(2)\n"
  "v_mfma_f32_32x32x16_bf16 v[64:79], a[8:11], v[132:135], v[64:79]\n"
  "ds_read2st64_b32 v[140:141], v38 offset0:12 offset1:13\n"
  "ds_read2st64_b32 v[142:143], v38 offset0:14 offset1:15\n"
  "buffer_load_dwordx4 v43, s[32:35], s39 offen lds\n"
  "v_mov_b32 v21, 0\n"
  "v_cvt_pk_bf16_f32 v15, v14, v14\n"
  "v_lshlrev_b32 v15, 16, v15\n"
  "v_mfma_f32_32x32x16_bf16 v[96:111], a[12:15], v[132:135], v[96:111]\n"
  "v_sub_f32 v15, v14, v15\n"
  "v_cvt_pk_bf16_f32 v18, v14, v15\n"
  "v_and_b32 v14, 0xffff0000, v18\n"
  "s_add_u32 s39, s41, 286720\n"
  "s_add_u32 m0, s37, 90112\n"
  "s_waitcnt lgkmcnt(2)\n"
  "v_mfma_f32_32x32x16_bf16 v[64:79], a[16:19], v[136:139], v[64:79]\n"
  "ds_read2st64_b32 v[128:129], v38 offset0:16 offset1:17\n"
  "ds_read2st64_b32 v[130:131], v38 offset0:18 offset1:19\n"
  "buffer_load_dwordx4 v43, s[28:31], s39 offen lds\n"
  "v_sub_f32 v15, v15, v14\n"
  "v_cvt_pk_bf16_f32 v19, v15, 0\n"
  "v_mul_f32 v16, v16, v63\n"
  "v_mfma_f32_32x32x16_bf16 v[96:111], a[20:23], v[136:139], v[96:111]\n"
  "v_mov_b32 v26, 0\n"
  "v_mov_b32 v27, 0\n"
  "v_cvt_pk_bf16_f32 v17, v16, v16\n"
  "v_lshlrev_b32 v17, 16, v17\n"
  "s_add_u32 m0, s37, 94208\n"
  "s_waitcnt lgkmcnt(2)\n"
  "v_mfma_f32_32x32x16_bf16 v[64:79], a[24:27], v[140:143], v[64:79]\n"
  "ds_read2st64_b32 v[132:133], v38 offset0:20 offset1:21\n"
  "ds_read2st64_b32 v[134:135], v38 offset0:22 offset1:23\n"
  "buffer_load_dwordx4 v43, s[32:35], s39 offen lds\n"
  "v_sub_f32 v17, v16, v17\n"
  "v_cvt_pk_bf16_f32 v24, v16, v17\n"
  "v_and_b32 v16, 0xffff0000, v24\n"
  "v_mfma_f32_32x32x16_bf16 v[96:111], a[28:31], v[140:143], v[96:111]\n"
  "v_sub_f32 v17, v17, v16\n"
  "v_cvt_pk_bf16_f32 v25, v17, 0\n"
  "v_mov_b32 v30, 0\n"
  "s_add_u32 s39, s41, 344064\n"
  "s_add_u32 m0, s37, 98304\n"
  "s_waitcnt lgkmcnt(2)\n"
  "v_mfma_f32_32x32x16_bf16 v[64:79], a[32:35], v[128:131], v[64:79]\n"
  "ds_read2st64_b32 v[136:137], v38 offset0:24 offset1:25\n"
  "ds_read2st64_b32 v[138:139], v38 offset0:26 offset1:27\n"
  "buffer_load_dwordx4 v43, s[28:31], s39 offen lds\n"
  "v_mov_b32 v31, 0\n"
  "s_mov_b32 s48, s54\n"
  "s_mov_b32 s49, s55\n"
  "v_mfma_f32_32x32x16_bf16 v[96:111], a[36:39], v[128:131], v[96:111]\n"
  "s_mov_b32 s50, 1605632\n"
  "s_mov_b32 s51, 0x20000\n"
  "v_and_b32 v10, 7, v1\n"
  "v_lshrrev_b32 v11, 3, v2\n"
  "s_add_u32 m0, s37, 102400\n"
  "s_waitcnt lgkmcnt(2)\n"
  "v_mfma_f32_32x32x16_bf16 v[64:79], a[40:43], v[132:135], v[64:79]\n"
  "ds_read2st64_b32 v[140:141], v38 offset0:28 offset1:29\n"
  "ds_read2st64_b32 v[142:143], v38 offset0:30 offset1:31\n"
  "buffer_load_dwordx4 v43, s[32:35], s39 offen lds\n"
  "s_lshr_b32 s47, s13, 2\n"
  "s_and_b32 s52, s12, 1\n"
  "s_sub_u32 s72, 159744, s38\n"
  "v_mfma_f32_32x32x16_bf16 v[96:111], a[44:47], v[132:135], v[96:111]\n"
  "v_mul_u32_u24 v39, 896, v11\n"
  "v_and_b32 v11, 6, v11\n"
  "v_xor_b32 v11, v11, v10\n"
  "s_add_u32 s39, s41, 401408\n"
  "s_add_u32 m0, s37, 106496\n"
  "s_waitcnt lgkmcnt(2)\n"
  "v_mfma_f32_32x32x16_bf16 v[64:79], a[48:51], v[136:139], v[64:79]\n"
  "ds_read2st64_b32 v[128:129], v38 offset0:32 offset1:33\n"
  "ds_read2st64_b32 v[130:131], v38 offset0:34 offset1:35\n"
  "buffer_load_dwordx4 v43, s[28:31], s39 offen lds\n"
  "v_lshl_add_u32 v39, v11, 4, v39\n"
  "s_lshl_b32 s4, s47, 7\n"
  "v_add_u32 v39, s4, v39\n"
  "v_mfma_f32_32x32x16_bf16 v[96:111], a[52:55], v[136:139], v[96:111]\n"
  "v_add_u32 v40, 7168, v39\n"
  "v_add_u32 v41, 14336, v39\n"
  "v_add_u32 v42, 21504, v39\n"
  "s_lshl_b32 s4, s47, 1\n"
  "s_add_u32 m0, s37, 110592\n"
  "s_waitcnt lgkmcnt(2)\n"
  "v_mfma_f32_32x32x16_bf16 v[64:79], a[56:59], v[140:143], v[64:79]\n"
  "ds_read2st64_b32 v[132:133], v38 offset0:36 offset1:37\n"
  "ds_read2st64_b32 v[134:135], v38 offset0:38 offset1:39\n"
  "buffer_load_dwordx4 v43, s[32:35], s39 offen lds\n"
  "s_mul_i32 s6, s3, 14\n"
  "s_add_u32 s53, s4, s6\n"
  "s_add_u32 s4, s4, 1\n"
  "v_mfma_f32_32x32x16_bf16 v[96:111], a[60:63], v[140:143], v[96:111]\n"
  "s_cmp_ge_u32 s4, 14\n"
  "s_cselect_b32 s4, 0, s4\n"
  "s_add_u32 s58, s4, s6\n"
  "s_add_u32 s39, s41, 458752\n"
  "s_add_u32 m0, s37, 114688\n"
  "s_waitcnt lgkmcnt(2)\n"
  "v_mfma_f32_32x32x16_bf16 v[64:79], a[64:67], v[128:131], v[64:79]\n"
  "ds_read2st64_b32 v[136:137], v38 offset0:40 offset1:41\n"
  "ds_read2st64_b32 v[138:139], v38 offset0:42 offset1:43\n"
  "buffer_load_dwordx4 v43, s[28:31], s39 offen lds\n"
  "s_add_u32 s4, s4, 1\n"
  "s_cmp_ge_u32 s4, 14\n"
  "s_cselect_b32 s4, 0, s4\n"
  "v_mfma_f32_32x32x16_bf16 v[96:111], a[68:71], v[128:131], v[96:111]\n"
  "s_add_u32 s59, s4, s6\n"
  "s_add_u32 s4, s4, 1\n"
  "s_cmp_ge_u32 s4, 14\n"
  "s_cselect_b32 s4, 0, s4\n"
  "s_add_u32 s60, s4, s6\n"
  "s_waitcnt lgkmcnt(2)\n"
  "v_mfma_f32_32x32x16_bf16 v[64:79], a[72:75], v[132:135], v[64:79]\n"
  "ds_read2st64_b32 v[140:141], v38 offset0:44 offset1:45\n"
  "ds_read2st64_b32 v[142:143], v38 offset0:46 offset1:47\n"
  "s_waitcnt vmcnt(9)\n"
  "s_barrier\n"
  "ds_read_b64_tr_b16 a[112:113], v13 offset:0\n"
  "ds_read_b64_tr_b16 a[114:115], v13 offset:2048\n"
  "s_add_u32 m0, s37, 118784\n"
  "v_mfma_f32_32x32x16_bf16 v[96:111], a[76:79], v[132:135], v[96:111]\n"
  "buffer_load_dwordx4 v43, s[32:35], s39 offen lds\n"
  "ds_read_b64_tr_b16 a[116:117], v13 offset:4096\n"
  "ds_read_b64_tr_b16 a[118:119], v13 offset:6144\n"
  "s_add_u32 s4, s4, 1\n"
  "s_waitcnt lgkmcnt(6)\n"
  "v_mfma_f32_32x32x16_bf16 v[64:79], a[80:83], v[136:139], v[64:79]\n"
  "ds_read2st64_b32 v[128:129], v38 offset0:48 offset1:49\n"
  "ds_read2st64_b32 v[130:131], v38 offset0:50 offset1:51\n"
  "ds_read_b64_tr_b16 a[120:121], v13 offset:8192\n"
  "ds_read_b64_tr_b16 a[122:123], v13 offset:10240\n"
  "s_add_u32 s39, s41, 516096\n"
  "s_add_u32 m0, s37, 122880\n"
  "v_mfma_f32_32x32x16_bf16 v[96:111], a[84:87], v[136:139], v[96:111]\n"
  "buffer_load_dwordx4 v43, s[28:31], s39 offen lds\n"
  "ds_read_b64_tr_b16 a[124:125], v13 offset:12288\n"
  "ds_read_b64_tr_b16 a[126:127], v13 offset:14336\n"
  "s_cmp_ge_u32 s4, 14\n"
  "s_cselect_b32 s4, 0, s4\n"
  "s_waitcnt lgkmcnt(10)\n"
  "v_mfma_f32_32x32x16_bf16 v[64:79], a[88:91], v[140:143], v[64:79]\n"
  "ds_read2st64_b32 v[132:133], v38 offset0:52 offset1:53\n"
  "ds_read2st64_b32 v[134:135], v38 offset0:54 offset1:55\n"
  "ds_read_b64_tr_b16 a[128:129], v13 offset:16384\n"
  "ds_read_b64_tr_b16 a[130:131], v13 offset:18432\n"
  "s_add_u32 s61, s4, s6\n"
  "s_add_u32 m0, s37, 126976\n"
  "v_mfma_f32_32x32x16_bf16 v[96:111], a[92:95], v[140:143], v[96:111]\n"
  "buffer_load_dwordx4 v43, s[32:35], s39 offen lds\n"
  "ds_read_b64_tr_b16 a[132:133], v13 offset:20480\n"
  "ds_read_b64_tr_b16 a[134:135], v13 offset:22528\n"
  "s_add_u32 s4, s4, 1\n"
  "s_waitcnt lgkmcnt(10)\n"
  "v_mfma_f32_32x32x16_bf16 v[64:79], a[96:99], v[128:131], v[64:79]\n"
  "ds_read2st64_b32 v[136:137], v38 offset0:56 offset1:57\n"
  "ds_read2st64_b32 v[138:139], v38 offset0:58 offset1:59\n"
  "ds_read_b64_tr_b16 a[136:137], v13 offset:24576\n"
  "ds_read_b64_tr_b16 a[138:139], v13 offset:26624\n"
  "s_add_u32 s39, s41, 573440\n"
  "s_add_u32 m0, s37, 131072\n"
  "v_mfma_f32_32x32x16_bf16 v[96:111], a[100:103], v[128:131], v[96:111]\n"
  "buffer_load_dwordx4 v43, s[28:31], s39 offen lds\n"
  "ds_read_b64_tr_b16 a[140:141], v13 offset:28672\n"
  "ds_read_b64_tr_b16 a[142:143], v13 offset:30720\n"
  "s_cmp_ge_u32 s4, 14\n"
  "s_cselect_b32 s4, 0, s4\n"
  "s_waitcnt lgkmcnt(10)\n"
  "v_mfma_f32_32x32x16_bf16 v[64:79], a[104:107], v[132:135], v[64:79]\n"
  "ds_read2st64_b32 v[140:141], v38 offset0:60 offset1:61\n"
  "ds_read2st64_b32 v[142:143], v38 offset0:62 offset1:63\n"
  "s_add_u32 s63, s4, s6\n"
  "s_add_u32 s4, s4, 1\n"
  "s_add_u32 m0, s37, 135168\n"
  "v_mfma_f32_32x32x16_bf16 v[96:111], a[108:111], v[132:135], v[96:111]\n"
  "buffer_load_dwordx4 v43, s[32:35], s39 offen lds\n"
  "s_cmp_ge_u32 s4, 14\n"
  "s_cselect_b32 s4, 0, s4\n"
  "s_add_u32 s64, s4, s6\n"
  "s_add_u32 s4, s4, 1\n"
  "s_waitcnt lgkmcnt(6)\n"
  "v_mfma_f32_32x32x16_bf16 v[64:79], a[112:115], v[136:139], v[64:79]\n"
  "ds_read2st64_b32 v[128:129], v38 offset0:64 offset1:65\n"
  "ds_read2st64_b32 v[130:131], v38 offset0:66 offset1:67\n"
  "s_cmp_ge_u32 s4, 14\n"
  "s_cselect_b32 s4, 0, s4\n"
  "s_add_u32 s39, s41, 630784\n"
  "s_add_u32 m0, s37, 139264\n"
  "v_mfma_f32_32x32x16_bf16 v[96:111], a[116:119], v[136:139], v[96:111]\n"
  "buffer_load_dwordx4 v43, s[28:31], s39 offen lds\n"
  "s_add_u32 s65, s4, s6\n"
  "s_add_u32 s4, s4, 1\n"
  "s_cmp_ge_u32 s4, 14\n"
  "s_cselect_b32 s4, 0, s4\n"
  "s_waitcnt lgkmcnt(2)\n"
  "v_mfma_f32_32x32x16_bf16 v[64:79], a[120:123], v[140:143], v[64:79]\n"
  "ds_read2st64_b32 v[132:133], v38 offset0:68 offset1:69\n"
  "ds_read2st64_b32 v[134:135], v38 offset0:70 offset1:71\n"
  "s_waitcnt vmcnt(7)\n"
  "s_barrier\n"
  "ds_read_b64_tr_b16 a[144:145], v13 offset:32768\n"
  "ds_read_b64_tr_b16 a[146:147], v13 offset:34816\n"
  "s_add_u32 m0, s37, 143360\n"
  "v_mfma_f32_32x32x16_bf16 v[96:111], a[124:127], v[140:143], v[96:111]\n"
  "buffer_load_dwordx4 v43, s[32:35], s39 offen lds\n"
  "ds_read_b64_tr_b16 a[148:149], v13 offset:36864\n"
  "ds_read_b64_tr_b16 a[150:151], v13 offset:38912\n"
  "s_add_u32 s66, s4, s6\n"
  "s_add_u32 s4, s4, 1\n"
  "s_waitcnt lgkmcnt(6)\n"
  "v_mfma_f32_32x32x16_bf16 v[64:79], a[128:131], v[128:131], v[64:79]\n"
  "ds_read2st64_b32 v[136:137], v38 offset0:72 offset1:73\n"
  "ds_read2st64_b32 v[138:139], v38 offset0:74 offset1:75\n"
  "ds_read_b64_tr_b16 a[152:153], v13 offset:40960\n"
  "ds_read_b64_tr_b16 a[154:155], v13 offset:43008\n"
  "s_add_u32 s39, s41, 688128\n"
  "s_add_u32 m0, s37, 147456\n"
  "v_mfma_f32_32x32x16_bf16 v[96:111], a[132:135], v[128:131], v[96:111]\n"
  "buffer_load_dwordx4 v43, s[28:31], s39 offen lds\n"
  "ds_read_b64_tr_b16 a[156:157], v13 offset:45056\n"
  "ds_read_b64_tr_b16 a[158:159], v13 offset:47104\n"
  "s_cmp_ge_u32 s4, 14\n"
  "s_cselect_b32 s4, 0, s4\n"
  "s_waitcnt lgkmcnt(10)\n"
  "v_mfma_f32_32x32x16_bf16 v[64:79], a[136:139], v[132:135], v[64:79]\n"
  "ds_read2st64_b32 v[140:141], v38 offset0:76 offset1:77\n"
  "ds_read2st64_b32 v[142:143], v38 offset0:78 offset1:79\n"
  "ds_read_b64_tr_b16 a[160:161], v13 offset:49152\n"
  "ds_read_b64_tr_b16 a[162:163], v13 offset:51200\n"
  "s_add_u32 s67, s4, s6\n"
  "s_add_u32 m0, s37, 151552\n"
  "v_mfma_f32_32x32x16_bf16 v[96:111], a[140:143], v[132:135], v[96:111]\n"
  "buffer_load_dwordx4 v43, s[32:35], s39 offen lds\n"
  "ds_read_b64_tr_b16 a[164:165], v13 offset:53248\n"
  "ds_read_b64_tr_b16 a[166:167], v13 offset:55296\n"
  "s_add_u32 s4, s4, 1\n"
  "s_waitcnt lgkmcnt(10)\n"
  "v_mfma_f32_32x32x16_bf16 v[64:79], a[144:147], v[136:139], v[64:79]\n"
  "ds_read2st64_b32 v[128:129], v38 offset0:80 offset1:81\n"
  "ds_read2st64_b32 v[130:131], v38 offset0:82 offset1:83\n"
  "ds_read_b64_tr_b16 a[168:169], v22 offset:0\n"
  "ds_read_b64_tr_b16 a[170:171], v22 offset:2048\n"
  "s_cmp_ge_u32 s4, 14\n"
  "s_cselect_b32 s4, 0, s4\n"
  "v_mfma_f32_32x32x16_bf16 v[96:111], a[148:151], v[136:139], v[96:111]\n"
  "ds_read_b64_tr_b16 a[172:173], v22 offset:4096\n"
  "ds_read_b64_tr_b16 a[174:175], v22 offset:6144\n"
  "s_add_u32 s68, s4, s6\n"
  "s_add_u32 s39, s41, 745472\n"
  "s_add_u32 m0, s37, 155648\n"
  "s_waitcnt lgkmcnt(10)\n"
  "v_mfma_f32_32x32x16_bf16 v[64:79], a[152:155], v[140:143], v[64:79]\n"
  "ds_read2st64_b32 v[132:133], v38 offset0:84 offset1:85\n"
  "ds_read2st64_b32 v[134:135], v38 offset0:86 offset1:87\n"
  "buffer_load_dwordx4 v43, s[28:31], s39 offen lds\n"
  "s_add_u32 s4, s4, 1\n"
  "s_cmp_ge_u32 s4, 14\n"
  "s_cselect_b32 s4, 0, s4\n"
  "v_mfma_f32_32x32x16_bf16 v[96:111], a[156:159], v[140:143], v[96:111]\n"
  "s_add_u32 s69, s4, s6\n"
  "s_add_u32 s4, s4, 1\n"
  "s_cmp_ge_u32 s4, 14\n"
  "s_cselect_b32 s4, 0, s4\n"
  "s_add_u32 m0, s37, 159744\n"
  "s_waitcnt lgkmcnt(6)\n"
  "v_mfma_f32_32x32x16_bf16 v[64:79], a[160:163], v[128:131], v[64:79]\n"
  "ds_read2st64_b32 v[136:137], v38 offset0:88 offset1:89\n"
  "ds_read2st64_b32 v[138:139], v38 offset0:90 offset1:91\n"
  "buffer_load_dwordx4 v43, s[32:35], s39 offen lds\n"
  "s_waitcnt vmcnt(6)\n"
  "s_barrier\n"
  "ds_read_b64_tr_b16 a[176:177], v22 offset:8192\n"
  "ds_read_b64_tr_b16 a[178:179], v22 offset:10240\n"
  "ds_read_b64_tr_b16 a[180:181], v22 offset:12288\n"
  "v_mfma_f32_32x32x16_bf16 v[96:111], a[164:167], v[128:131], v[96:111]\n"
  "ds_read_b64_tr_b16 a[182:183], v22 offset:14336\n"
  "ds_read_b64_tr_b16 a[184:185], v22 offset:16384\n"
  "ds_read_b64_tr_b16 a[186:187], v22 offset:18432\n"
  "v_add_u32 v23, 128, v23\n"
  "s_add_u32 s39, s23, 0\n"
  "s_add_u32 m0, s43, 57344\n"
  "s_waitcnt lgkmcnt(8)\n"
  "v_mfma_f32_32x32x16_bf16 v[64:79], a[168:171], v[132:135], v[64:79]\n"
  "ds_read2st64_b32 v[140:141], v38 offset0:92 offset1:93\n"
  "ds_read2st64_b32 v[142:143], v38 offset0:94 offset1:95\n"
  "buffer_load_dwordx4 v23, s[24:27], s39 offen sc1 lds\n"
  "ds_read_b64_tr_b16 a[188:189], v22 offset:20480\n"
  "ds_read_b64_tr_b16 a[190:191], v22 offset:22528\n"
  "ds_read_b64_tr_b16 a[192:193], v22 offset:24576\n"
  "v_mfma_f32_32x32x16_bf16 v[96:111], a[172:175], v[132:135], v[96:111]\n"
  "ds_read_b64_tr_b16 a[194:195], v22 offset:26624\n"
  "ds_read_b64_tr_b16 a[196:197], v22 offset:28672\n"
  "ds_read_b64_tr_b16 a[198:199], v22 offset:30720\n"
  "s_add_u32 s39, s23, 65536\n"
  "s_add_u32 m0, s43, 58368\n"
  "s_waitcnt lgkmcnt(10)\n"
  "v_mfma_f32_32x32x16_bf16 v[64:79], a[176:179], v[136:139], v[64:79]\n"
  "ds_read2st64_b32 v[128:129], v38 offset0:96 offset1:97\n"
  "ds_read2st64_b32 v[130:131], v38 offset0:98 offset1:99\n"
  "buffer_load_dwordx4 v23, s[24:27], s39 offen sc1 lds\n"
  "s_add_u32 s70, s4, s6\n"
  "s_add_u32 s4, s4, 1\n"
  "v_mfma_f32_32x32x16_bf16 v[96:111], a[180:183], v[136:139], v[96:111]\n"
  "s_cmp_ge_u32 s4, 14\n"
  "s_cselect_b32 s4, 0, s4\n"
  "s_add_u32 s71, s4, s6\n"
  "s_add_u32 s39, s23, 131072\n"
  "s_add_u32 m0, s43, 59392\n"
  "s_waitcnt lgkmcnt(6)\n"
  "v_mfma_f32_32x32x16_bf16 v[64:79], a[184:187], v[140:143], v[64:79]\n"
  "ds_read2st64_b32 v[132:133], v38 offset0:100 offset1:101\n"
  "ds_read2st64_b32 v[134:135], v38 offset0:102 offset1:103\n"
  "buffer_load_dwordx4 v23, s[24:27], s39 offen sc1 lds\n"
  "v_bfe_u32 v10, v2, 2, 2\n"
  "v_and_b32 v11, 1, v37\n"
  "v_lshl_add_u32 v11, v11, 2, v10\n"
  "v_mfma_f32_32x32x16_bf16 v[96:111], a[188:191], v[140:143], v[96:111]\n"
  "v_bfe_u32 v10, v11, 1, 2\n"
  "v_xor_b32 v10, s3, v10\n"
  "v_lshl_add_u32 v11, v4, 4, v11\n"
  "s_add_u32 s39, s23, 196608\n"
  "s_add_u32 m0, s43, 60416\n"
  "s_waitcnt lgkmcnt(2)\n"
  "v_mfma_f32_32x32x16_bf16 v[64:79], a[192:195], v[128:131], v[64:79]\n"
  "ds_read2st64_b32 v[136:137], v38 offset0:104 offset1:105\n"
  "ds_read2st64_b32 v[138:139], v38 offset0:106 offset1:107\n"
  "buffer_load_dwordx4 v23, s[24:27], s39 offen sc1 lds\n"
  "v_lshlrev_b32 v11, 7, v11\n"
  "v_lshl_add_u32 v11, v8, 3, v11\n"
  "v_lshl_add_u32 v11, v10, 5, v11\n"
  "v_mfma_f32_32x32x16_bf16 v[96:111], a[196:199], v[128:131], v[96:111]\n"
  "s_waitcnt vmcnt(4)\n"
  "s_barrier\n"
  "ds_read_b64_tr_b16 a[200:201], v22 offset:32768\n"
  "ds_read_b64_tr_b16 a[202:203], v22 offset:34816\n"
  "ds_read_b64_tr_b16 a[204:205], v22 offset:36864\n"
  "ds_read_b64_tr_b16 a[206:207], v22 offset:38912\n"
  "s_add_u32 s39, s23, 262144\n"
  "s_add_u32 m0, s43, 61440\n"
  "s_waitcnt lgkmcnt(0)\n"
  "v_mfma_f32_32x32x16_bf16 v[64:79], a[200:203], v[132:135], v[64:79]\n"
  "ds_read2st64_b32 v[140:141], v38 offset0:108 offset1:109\n"
  "ds_read2st64_b32 v[142:143], v38 offset0:110 offset1:111\n"
  "buffer_load_dwordx4 v23, s[24:27], s39 offen sc1 lds\n"
  "ds_read_b64_tr_b16 a[208:209], v22 offset:40960\n"
  "ds_read_b64_tr_b16 a[210:211], v22 offset:43008\n"
  "ds_read_b64_tr_b16 a[212:213], v22 offset:45056\n"
  "ds_read_b64_tr_b16 a[214:215], v22 offset:47104\n"
  "v_mfma_f32_32x32x16_bf16 v[96:111], a[204:207], v[132:135], v[96:111]\n"
  "ds_read_b64_tr_b16 a[216:217], v22 offset:49152\n"
  "ds_read_b64_tr_b16 a[218:219], v22 offset:51200\n"
  "ds_read_b64_tr_b16 a[220:221], v22 offset:53248\n"
  "ds_read_b64_tr_b16 a[222:223], v22 offset:55296\n"
  "s_add_u32 s39, s23, 327680\n"
  "s_add_u32 m0, s43, 62464\n"
  "s_waitcnt lgkmcnt(4)\n"
  "v_mfma_f32_32x32x16_bf16 v[64:79], a[208:211], v[136:139], v[64:79]\n"
  "buffer_load_dwordx4 v23, s[24:27], s39 offen sc1 lds\n"
  "s_sub_u32 s4, s72, 49152\n"
  "s_sub_u32 s5, s4, 65536\n"
  "v_xor_b32 v10, 0, v11\n"
  "v_add_u32 v46, s4, v10\n"
  "v_add_u32 v52, s5, v10\n"
  "v_mfma_f32_32x32x16_bf16 v[96:111], a[212:215], v[136:139], v[96:111]\n"
  "buffer_load_dwordx2 v[32:33], v7, s[24:27], 0 offen sc1\n"
  "buffer_load_dwordx2 v[34:35], v7, s[24:27], 0 offen offset:256 sc1\n"
  "v_xor_b32 v10, 32, v11\n"
  "v_add_u32 v47, s4, v10\n"
  "v_add_u32 v53, s5, v10\n"
  "s_waitcnt lgkmcnt(0)\n"
  "v_mfma_f32_32x32x16_bf16 v[64:79], a[216:219], v[140:143], v[64:79]\n"
  "v_xor_b32 v10, 64, v11\n"
  "v_add_u32 v50, s4, v10\n"
  "v_add_u32 v54, s5, v10\n"
  "v_xor_b32 v10, 96, v11\n"
  "s_add_u32 s39, s23, 393216\n"
  "s_add_u32 m0, s43, 63488\n"
  "v_mfma_f32_32x32x16_bf16 v[96:111], a[220:223], v[140:143], v[96:111]\n"
  "buffer_load_dwordx4 v23, s[24:27], s39 offen sc1 lds\n"
  "s_mov_b64 exec, s[44:45]\n"
  "buffer_load_dword v5, v45, s[24:27], 0 offen offset:128 sc1\n"
  "s_mov_b64 exec, -1\n"
  "v_add_u32 v51, s4, v10\n"
  "v_add_u32 v55, s5, v10\n"
  "s_mul_i32 s4, s16, 458752\n"
  "s_mul_i32 s5, s52, 57344\n"
  "s_add_u32 s4, s4, s5\n"
  "s_add_u32 s4, s4, 7340032\n"
  "v_lshl_add_u32 v56, v2, 4, s4\n"
  "s_lshl_b32 s4, s16, 6\n"
  "s_lshl_b32 s5, s52, 4\n"
  "s_add_u32 s4, s4, s5\n"
  "v_add_u32 v10, s4, v36\n"
  "s_lshl_b32 s4, s47, 2\n"
  "s_add_u32 s4, s4, s3\n"
  "s_lshl_b32 s5, s4, 16\n"
  "s_add_u32 s5, s5, 5242880\n"
  "v_lshlrev_b32 v57, 14, v37\n"
  "v_lshl_add_u32 v57, v10, 2, v57\n"
  "v_add_u32 v57, s5, v57\n"
  "s_lshl_b32 s4, s4, 5\n"
  "v_mul_u32_u24 v58, 896, v10\n"
  "v_lshl_add_u32 v58, v37, 3, v58\n"
  "v_add_u32 v58, s4, v58\n"
  "v_lshl_add_u32 v62, v2, 4, s38\n"
  "v_xor_b32 v59, 4096, v62\n"
  "v_xor_b32 v60, 8192, v62\n"
  "v_xor_b32 v61, 12288, v62\n"
  "s_waitcnt vmcnt(0)\n"
  "L23_%=:\n"
  "v_cmp_gt_u32 vcc, 28, v5\n"
  "s_nop 0\n"
  "s_and_b64 vcc, exec, vcc\n"
  "s_cbranch_vccnz L24_%=\n"
  "L25_%=:\n"
  "s_barrier\n"
  "v_add_u32 v38, 57344, v38\n"
  "ds_read2st64_b32 v[128:129], v38 offset0:0 offset1:1\n"
  "ds_read2st64_b32 v[130:131], v38 offset0:2 offset1:3\n"
  "ds_read2st64_b32 v[132:133], v38 offset0:4 offset1:5\n"
  "ds_read2st64_b32 v[134:135], v38 offset0:6 offset1:7\n"
  "s_mul_i32 s22, s53, 28672\n"
  "s_sub_u32 m0, s72, 0\n"
  "s_waitcnt lgkmcnt(2)\n"
  "v_mfma_f32_32x32x16_bf16 v[80:95], a[0:3], v[128:131], 0\n"
  "ds_read2st64_b32 v[136:137], v38 offset0:8 offset1:9\n"
  "ds_read2st64_b32 v[138:139], v38 offset0:10 offset1:11\n"
  "buffer_load_dwordx4 v39, s[48:51], s22 offen lds\n"
  "L26_%=:\n"
  "v_xor_b32 v10, s8, v33\n"
  "v_bitop3_b32 v10, v10, v35, s8 bitop3:0xf6\n"
  "v_cmp_ne_u32 vcc, 0, v10\n"
  "s_nop 0\n"
  "s_and_b64 vcc, exec, vcc\n"
  "s_cbranch_vccnz L27_%=\n"
  "L28_%=:\n"
  "v_mfma_f32_32x32x16_bf16 v[112:127], a[4:7], v[128:131], 0\n"
  "v_xor_b32 v63, 0x8000, v32\n"
  "v_and_b32 v145, 0xffff0000, v32\n"
  "v_and_b32 v29, 0xffff, v63\n"
  "s_sub_u32 m0, s72, -1024\n"
  "s_waitcnt lgkmcnt(2)\n"
  "v_mfma_f32_32x32x16_bf16 v[80:95], a[8:11], v[132:135], v[80:95]\n"
  "ds_read2st64_b32 v[140:141], v38 offset0:12 offset1:13\n"
  "ds_read2st64_b32 v[142:143], v38 offset0:14 offset1:15\n"
  "buffer_load_dwordx4 v40, s[48:51], s22 offen lds\n"
  "v_rcp_f32 v144, v145\n"
  "v_lshl_or_b32 v28, v29, 16, v29\n"
  "v_mfma_f32_32x32x16_bf16 v[112:127], a[12:15], v[132:135], v[112:127]\n"
  "v_mul_f32 v145, v145, v145\n"
  "v_mul_f32 v144, 0xbfb8aa3b, v144\n"
  "v_mfma_f32_32x32x16_bf16 v[64:79], v[18:21], v[28:31], v[64:79]\n"
  "v_mfma_f32_32x32x16_bf16 v[96:111], v[24:27], v[28:31], v[96:111]\n"
  "s_sub_u32 m0, s72, -2048\n"
  "s_waitcnt lgkmcnt(2)\n"
  "v_mfma_f32_32x32x16_bf16 v[80:95], a[16:19], v[136:139], v[80:95]\n"
  "ds_read2st64_b32 v[128:129], v38 offset0:16 offset1:17\n"
  "ds_read2st64_b32 v[130:131], v38 offset0:18 offset1:19\n"
  "buffer_load_dwordx4 v41, s[48:51], s22 offen lds\n"
  "v_mfma_f32_32x32x16_bf16 v[112:127], a[20:23], v[136:139], v[112:127]\n"
  "s_nop 3\n"
  "v_mul_f32 v63, v144, v64\n"
  "v_mul_f32 v146, v144, v65\n"
  "v_exp_f32 v63, v63\n"
  "s_sub_u32 m0, s72, -3072\n"
  "s_waitcnt lgkmcnt(2)\n"
  "v_mfma_f32_32x32x16_bf16 v[80:95], a[24:27], v[140:143], v[80:95]\n"
  "ds_read2st64_b32 v[132:133], v38 offset0:20 offset1:21\n"
  "ds_read2st64_b32 v[134:135], v38 offset0:22 offset1:23\n"
  "buffer_load_dwordx4 v42, s[48:51], s22 offen lds\n"
  "v_mul_f32 v64, v64, v96\n"
  "v_mul_f32 v147, v144, v66\n"
  "v_mfma_f32_32x32x16_bf16 v[112:127], a[28:31], v[140:143], v[112:127]\n"
  "v_exp_f32 v146, v146\n"
  "v_mul_f32 v65, v65, v97\n"
  "v_fma_f32 v148, v63, v145, v145\n"
  "s_waitcnt lgkmcnt(2)\n"
  "v_mfma_f32_32x32x16_bf16 v[80:95], a[32:35], v[128:131], v[80:95]\n"
  "ds_read2st64_b32 v[136:137], v38 offset0:24 offset1:25\n"
  "ds_read2st64_b32 v[138:139], v38 offset0:26 offset1:27\n"
  "v_add_u32 v23, 3968, v23\n"
  "s_add_u32 s39, s23, 0\n"
  "s_add_u32 m0, s43, 28672\n"
  "v_mfma_f32_32x32x16_bf16 v[112:127], a[36:39], v[128:131], v[112:127]\n"
  "buffer_load_dwordx4 v23, s[24:27], s39 offen sc1 lds\n"
  "v_rcp_f32 v148, v148\n"
  "v_mul_f32 v63, v144, v67\n"
  "s_waitcnt lgkmcnt(2)\n"
  "v_mfma_f32_32x32x16_bf16 v[80:95], a[40:43], v[132:135], v[80:95]\n"
  "ds_read2st64_b32 v[140:141], v38 offset0:28 offset1:29\n"
  "ds_read2st64_b32 v[142:143], v38 offset0:30 offset1:31\n"
  "v_exp_f32 v147, v147\n"
  "s_add_u32 s39, s23, 65536\n"
  "s_add_u32 m0, s43, 29696\n"
  "v_mfma_f32_32x32x16_bf16 v[112:127], a[44:47], v[132:135], v[112:127]\n"
  "buffer_load_dwordx4 v23, s[24:27], s39 offen sc1 lds\n"
  "v_mul_f32 v66, v66, v98\n"
  "v_fma_f32 v149, v146, v145, v145\n"
  "v_rcp_f32 v149, v149\n"
  "s_waitcnt lgkmcnt(2)\n"
  "v_mfma_f32_32x32x16_bf16 v[80:95], a[48:51], v[136:139], v[80:95]\n"
  "ds_read2st64_b32 v[128:129], v38 offset0:32 offset1:33\n"
  "ds_read2st64_b32 v[130:131], v38 offset0:34 offset1:35\n"
  "v_mul_f32 v64, v64, v148\n"
  "v_mul_f32 v146, v144, v68\n"
  "s_add_u32 s39, s23, 131072\n"
  "s_add_u32 m0, s43, 30720\n"
  "v_mfma_f32_32x32x16_bf16 v[112:127], a[52:55], v[136:139], v[112:127]\n"
  "buffer_load_dwordx4 v23, s[24:27], s39 offen sc1 lds\n"
  "v_exp_f32 v63, v63\n"
  "v_mul_f32 v67, v67, v99\n"
  "v_fma_f32 v150, v147, v145, v145\n"
  "s_waitcnt lgkmcnt(2)\n"
  "v_mfma_f32_32x32x16_bf16 v[80:95], a[56:59], v[140:143], v[80:95]\n"
  "ds_read2st64_b32 v[132:133], v38 offset0:36 offset1:37\n"
  "ds_read2st64_b32 v[134:135], v38 offset0:38 offset1:39\n"
  "v_rcp_f32 v150, v150\n"
  "s_add_u32 s39, s23, 196608\n"
  "s_add_u32 m0, s43, 31744\n"
  "v_mfma_f32_32x32x16_bf16 v[112:127], a[60:63], v[140:143], v[112:127]\n"
  "buffer_load_dwordx4 v23, s[24:27], s39 offen sc1 lds\n"
  "v_mul_f32 v65, v65, v149\n"
  "v_cvt_pk_bf16_f32 v96, v64, v65\n"
  "v_mul_f32 v147, v144, v69\n"
  "s_waitcnt lgkmcnt(2)\n"
  "v_mfma_f32_32x32x16_bf16 v[80:95], a[64:67], v[128:131], v[80:95]\n"
  "ds_read2st64_b32 v[136:137], v38 offset0:40 offset1:41\n"
  "ds_read2st64_b32 v[138:139], v38 offset0:42 offset1:43\n"
  "v_exp_f32 v146, v146\n"
  "v_mul_f32 v68, v68, v100\n"
  "v_fma_f32 v148, v63, v145, v145\n"
  "v_mfma_f32_32x32x16_bf16 v[112:127], a[68:71], v[128:131], v[112:127]\n"
  "v_rcp_f32 v148, v148\n"
  "v_mul_f32 v66, v66, v150\n"
  "s_add_u32 s39, s23, 262144\n"
  "s_add_u32 m0, s43, 32768\n"
  "s_waitcnt lgkmcnt(2)\n"
  "v_mfma_f32_32x32x16_bf16 v[80:95], a[72:75], v[132:135], v[80:95]\n"
  "ds_read2st64_b32 v[140:141], v38 offset0:44 offset1:45\n"
  "ds_read2st64_b32 v[142:143], v38 offset0:46 offset1:47\n"
  "buffer_load_dwordx4 v23, s[24:27], s39 offen sc1 lds\n"
  "v_mul_f32 v63, v144, v70\n"
  "v_exp_f32 v147, v147\n"
  "v_mfma_f32_32x32x16_bf16 v[112:127], a[76:79], v[132:135], v[112:127]\n"
  "v_mul_f32 v69, v69, v101\n"
  "v_fma_f32 v149, v146, v145, v145\n"
  "s_add_u32 s39, s23, 327680\n"
  "s_add_u32 m0, s43, 33792\n"
  "s_waitcnt lgkmcnt(2)\n"
  "v_mfma_f32_32x32x16_bf16 v[80:95], a[80:83], v[136:139], v[80:95]\n"
  "ds_read2st64_b32 v[128:129], v38 offset0:48 offset1:49\n"
  "ds_read2st64_b32 v[130:131], v38 offset0:50 offset1:51\n"
  "buffer_load_dwordx4 v23, s[24:27], s39 offen sc1 lds\n"
  "v_rcp_f32 v149, v149\n"
  "v_mul_f32 v67, v67, v148\n"
  "v_mfma_f32_32x32x16_bf16 v[112:127], a[84:87], v[136:139], v[112:127]\n"
  "v_cvt_pk_bf16_f32 v97, v66, v67\n"
  "v_mul_f32 v146, v144, v71\n"
  "s_add_u32 s39, s23, 393216\n"
  "s_add_u32 m0, s43, 34816\n"
  "s_waitcnt lgkmcnt(2)\n"
  "v_mfma_f32_32x32x16_bf16 v[80:95], a[88:91], v[140:143], v[80:95]\n"
  "ds_read2st64_b32 v[132:133], v38 offset0:52 offset1:53\n"
  "ds_read2st64_b32 v[134:135], v38 offset0:54 offset1:55\n"
  "buffer_load_dwordx4 v23, s[24:27], s39 offen sc1 lds\n"
  "v_exp_f32 v63, v63\n"
  "v_mul_f32 v70, v70, v102\n"
  "v_mfma_f32_32x32x16_bf16 v[112:127], a[92:95], v[140:143], v[112:127]\n"
  "v_fma_f32 v150, v147, v145, v145\n"
  "v_rcp_f32 v150, v150\n"
  "v_mul_f32 v68, v68, v149\n"
  "v_mul_f32 v147, v144, v72\n"
  "s_waitcnt lgkmcnt(2)\n"
  "v_mfma_f32_32x32x16_bf16 v[80:95], a[96:99], v[128:131], v[80:95]\n"
  "ds_read2st64_b32 v[136:137], v38 offset0:56 offset1:57\n"
  "ds_read2st64_b32 v[138:139], v38 offset0:58 offset1:59\n"
  "s_waitcnt vmcnt(7)\n"
  "ds_read_b64_tr_b16 a[224:225], v46 offset:49152\n"
  "ds_read_b64_tr_b16 a[226:227], v46 offset:50176\n"
  "s_mul_i32 s22, s58, 28672\n"
  "s_sub_u32 m0, s72, 16384\n"
  "v_mfma_f32_32x32x16_bf16 v[112:127], a[100:103], v[128:131], v[112:127]\n"
  "buffer_load_dwordx4 v39, s[48:51], s22 offen lds\n"
  "ds_read_b64_tr_b16 a[228:229], v47 offset:49152\n"
  "ds_read_b64_tr_b16 a[230:231], v47 offset:50176\n"
  "v_exp_f32 v146, v146\n"
  "s_waitcnt lgkmcnt(6)\n"
  "v_mfma_f32_32x32x16_bf16 v[80:95], a[104:107], v[132:135], v[80:95]\n"
  "ds_read2st64_b32 v[140:141], v38 offset0:60 offset1:61\n"
  "ds_read2st64_b32 v[142:143], v38 offset0:62 offset1:63\n"
  "ds_read_b64_tr_b16 a[232:233], v50 offset:49152\n"
  "ds_read_b64_tr_b16 a[234:235], v50 offset:50176\n"
  "v_mul_f32 v71, v71, v103\n"
  "s_sub_u32 m0, s72, 15360\n"
  "v_mfma_f32_32x32x16_bf16 v[112:127], a[108:111], v[132:135], v[112:127]\n"
  "buffer_load_dwordx4 v40, s[48:51], s22 offen lds\n"
  "ds_read_b64_tr_b16 a[236:237], v51 offset:49152\n"
  "ds_read_b64_tr_b16 a[238:239], v51 offset:50176\n"
  "v_fma_f32 v148, v63, v145, v145\n"
  "s_waitcnt lgkmcnt(10)\n"
  "v_mfma_f32_32x32x16_bf16 v[80:95], a[112:115], v[136:139], v[80:95]\n"
  "ds_read2st64_b32 v[128:129], v38 offset0:64 offset1:65\n"
  "ds_read2st64_b32 v[130:131], v38 offset0:66 offset1:67\n"
  "v_rcp_f32 v148, v148\n"
  "v_mul_f32 v69, v69, v150\n"
  "s_sub_u32 m0, s72, 14336\n"
  "v_mfma_f32_32x32x16_bf16 v[112:127], a[116:119], v[136:139], v[112:127]\n"
  "buffer_load_dwordx4 v41, s[48:51], s22 offen lds\n"
  "v_cvt_pk_bf16_f32 v98, v68, v69\n"
  "v_mul_f32 v63, v144, v73\n"
  "v_exp_f32 v147, v147\n"
  "s_waitcnt lgkmcnt(6)\n"
  "v_mfma_f32_32x32x16_bf16 v[80:95], a[120:123], v[140:143], v[80:95]\n"
  "ds_read2st64_b32 v[132:133], v38 offset0:68 offset1:69\n"
  "ds_read2st64_b32 v[134:135], v38 offset0:70 offset1:71\n"
  "v_mul_f32 v72, v72, v104\n"
  "v_fma_f32 v149, v146, v145, v145\n"
  "s_sub_u32 m0, s72, 13312\n"
  "v_mfma_f32_32x32x16_bf16 v[112:127], a[124:127], v[140:143], v[112:127]\n"
  "buffer_load_dwordx4 v42, s[48:51], s22 offen lds\n"
  "v_rcp_f32 v149, v149\n"
  "v_mul_f32 v70, v70, v148\n"
  "v_mul_f32 v146, v144, v74\n"
  "s_waitcnt lgkmcnt(2)\n"
  "v_mfma_f32_32x32x16_bf16 v[80:95], a[128:131], v[128:131], v[80:95]\n"
  "ds_read2st64_b32 v[136:137], v38 offset0:72 offset1:73\n"
  "ds_read2st64_b32 v[138:139], v38 offset0:74 offset1:75\n"
  "v_exp_f32 v63, v63\n"
  "v_mul_f32 v73, v73, v105\n"
  "v_fma_f32 v150, v147, v145, v145\n"
  "v_mfma_f32_32x32x16_bf16 v[112:127], a[132:135], v[128:131], v[112:127]\n"
  "v_rcp_f32 v150, v150\n"
  "v_mul_f32 v71, v71, v149\n"
  "s_mul_i32 s22, s59, 28672\n"
  "s_sub_u32 m0, s72, 32768\n"
  "s_waitcnt lgkmcnt(2)\n"
  "v_mfma_f32_32x32x16_bf16 v[80:95], a[136:139], v[132:135], v[80:95]\n"
  "ds_read2st64_b32 v[140:141], v38 offset0:76 offset1:77\n"
  "ds_read2st64_b32 v[142:143], v38 offset0:78 offset1:79\n"
  "buffer_load_dwordx4 v39, s[48:51], s22 offen lds\n"
  "v_cvt_pk_bf16_f32 v99, v70, v71\n"
  "v_mul_f32 v147, v144, v75\n"
  "v_mfma_f32_32x32x16_bf16 v[112:127], a[140:143], v[132:135], v[112:127]\n"
  "v_exp_f32 v146, v146\n"
  "v_mul_f32 v74, v74, v106\n"
  "v_fma_f32 v148, v63, v145, v145\n"
  "s_sub_u32 m0, s72, 31744\n"
  "s_waitcnt lgkmcnt(2)\n"
  "v_mfma_f32_32x32x16_bf16 v[80:95], a[144:147], v[136:139], v[80:95]\n"
  "ds_read2st64_b32 v[128:129], v38 offset0:80 offset1:81\n"
  "ds_read2st64_b32 v[130:131], v38 offset0:82 offset1:83\n"
  "buffer_load_dwordx4 v40, s[48:51], s22 offen lds\n"
  "v_rcp_f32 v148, v148\n"
  "v_mul_f32 v72, v72, v150\n"
  "v_mfma_f32_32x32x16_bf16 v[112:127], a[148:151], v[136:139], v[112:127]\n"
  "v_mul_f32 v63, v144, v76\n"
  "v_exp_f32 v147, v147\n"
  "v_mul_f32 v75, v75, v107\n"
  "s_sub_u32 m0, s72, 30720\n"
  "s_waitcnt lgkmcnt(2)\n"
  "v_mfma_f32_32x32x16_bf16 v[80:95], a[152:155], v[140:143], v[80:95]\n"
  "ds_read2st64_b32 v[132:133], v38 offset0:84 offset1:85\n"
  "ds_read2st64_b32 v[134:135], v38 offset0:86 offset1:87\n"
  "buffer_load_dwordx4 v41, s[48:51], s22 offen lds\n"
  "v_fma_f32 v149, v146, v145, v145\n"
  "v_rcp_f32 v149, v149\n"
  "v_mfma_f32_32x32x16_bf16 v[112:127], a[156:159], v[140:143], v[112:127]\n"
  "v_mul_f32 v73, v73, v148\n"
  "v_cvt_pk_bf16_f32 v100, v72, v73\n"
  "v_mul_f32 v146, v144, v77\n"
  "s_sub_u32 m0, s72, 29696\n"
  "s_waitcnt lgkmcnt(2)\n"
  "v_mfma_f32_32x32x16_bf16 v[80:95], a[160:163], v[128:131], v[80:95]\n"
  "ds_read2st64_b32 v[136:137], v38 offset0:88 offset1:89\n"
  "ds_read2st64_b32 v[138:139], v38 offset0:90 offset1:91\n"
  "buffer_load_dwordx4 v42, s[48:51], s22 offen lds\n"
  "v_exp_f32 v63, v63\n"
  "v_mul_f32 v76, v76, v108\n"
  "v_mfma_f32_32x32x16_bf16 v[112:127], a[164:167], v[128:131], v[112:127]\n"
  "v_fma_f32 v150, v147, v145, v145\n"
  "v_rcp_f32 v150, v150\n"
  "v_mul_f32 v74, v74, v149\n"
  "v_mul_f32 v147, v144, v78\n"
  "s_waitcnt lgkmcnt(2)\n"
  "v_mfma_f32_32x32x16_bf16 v[80:95], a[168:171], v[132:135], v[80:95]\n"
  "ds_read2st64_b32 v[140:141], v38 offset0:92 offset1:93\n"
  "ds_read2st64_b32 v[142:143], v38 offset0:94 offset1:95\n"
  "v_exp_f32 v146, v146\n"
  "s_mul_i32 s22, s60, 28672\n"
  "s_sub_u32 m0, s72, 49152\n"
  "v_mfma_f32_32x32x16_bf16 v[112:127], a[172:175], v[132:135], v[112:127]\n"
  "buffer_load_dwordx4 v39, s[48:51], s22 offen lds\n"
  "v_mul_f32 v77, v77, v109\n"
  "v_fma_f32 v148, v63, v145, v145\n"
  "v_rcp_f32 v148, v148\n"
  "s_waitcnt lgkmcnt(2)\n"
  "v_mfma_f32_32x32x16_bf16 v[80:95], a[176:179], v[136:139], v[80:95]\n"
  "ds_read2st64_b32 v[128:129], v38 offset0:96 offset1:97\n"
  "ds_read2st64_b32 v[130:131], v38 offset0:98 offset1:99\n"
  "s_waitcnt vmcnt(5)\n"
  "ds_read_b64_tr_b16 a[240:241], v46 offset:32768\n"
  "ds_read_b64_tr_b16 a[242:243], v46 offset:33792\n"
  "s_sub_u32 m0, s72, 48128\n"
  "v_mfma_f32_32x32x16_bf16 v[112:127], a[180:183], v[136:139], v[112:127]\n"
  "buffer_load_dwordx4 v40, s[48:51], s22 offen lds\n"
  "ds_read_b64_tr_b16 a[244:245], v47 offset:32768\n"
  "ds_read_b64_tr_b16 a[246:247], v47 offset:33792\n"
  "v_mul_f32 v75, v75, v150\n"
  "v_cvt_pk_bf16_f32 v101, v74, v75\n"
  "s_waitcnt lgkmcnt(6)\n"
  "v_mfma_f32_32x32x16_bf16 v[80:95], a[184:187], v[140:143], v[80:95]\n"
  "ds_read2st64_b32 v[132:133], v38 offset0:100 offset1:101\n"
  "ds_read2st64_b32 v[134:135], v38 offset0:102 offset1:103\n"
  "ds_read_b64_tr_b16 a[248:249], v50 offset:32768\n"
  "ds_read_b64_tr_b16 a[250:251], v50 offset:33792\n"
  "v_mul_f32 v63, v144, v79\n"
  "s_sub_u32 m0, s72, 47104\n"
  "v_mfma_f32_32x32x16_bf16 v[112:127], a[188:191], v[140:143], v[112:127]\n"
  "buffer_load_dwordx4 v41, s[48:51], s22 offen lds\n"
  "ds_read_b64_tr_b16 a[252:253], v51 offset:32768\n"
  "ds_read_b64_tr_b16 a[254:255], v51 offset:33792\n"
  "v_exp_f32 v147, v147\n"
  "s_waitcnt lgkmcnt(10)\n"
  "v_mfma_f32_32x32x16_bf16 v[80:95], a[192:195], v[128:131], v[80:95]\n"
  "ds_read2st64_b32 v[136:137], v38 offset0:104 offset1:105\n"
  "ds_read2st64_b32 v[138:139], v38 offset0:106 offset1:107\n"
  "v_mul_f32 v78, v78, v110\n"
  "v_fma_f32 v149, v146, v145, v145\n"
  "v_rcp_f32 v149, v149\n"
  "v_mfma_f32_32x32x16_bf16 v[112:127], a[196:199], v[128:131], v[112:127]\n"
  "v_mul_f32 v76, v76, v148\n"
  "v_exp_f32 v63, v63\n"
  "v_mul_f32 v79, v79, v111\n"
  "s_sub_u32 m0, s72, 46080\n"
  "s_waitcnt lgkmcnt(6)\n"
  "v_mfma_f32_32x32x16_bf16 v[80:95], a[200:203], v[132:135], v[80:95]\n"
  "ds_read2st64_b32 v[140:141], v38 offset0:108 offset1:109\n"
  "ds_read2st64_b32 v[142:143], v38 offset0:110 offset1:111\n"
  "buffer_load_dwordx4 v42, s[48:51], s22 offen lds\n"
  "s_waitcnt vmcnt(12)\n"
  "s_barrier\n"
  "v_fma_f32 v150, v147, v145, v145\n"
  "v_mfma_f32_32x32x16_bf16 v[112:127], a[204:207], v[132:135], v[112:127]\n"
  "v_rcp_f32 v150, v150\n"
  "v_mul_f32 v77, v77, v149\n"
  "v_cvt_pk_bf16_f32 v102, v76, v77\n"
  "v_fma_f32 v148, v63, v145, v145\n"
  "s_waitcnt lgkmcnt(2)\n"
  "v_mfma_f32_32x32x16_bf16 v[80:95], a[208:211], v[136:139], v[80:95]\n"
  "v_add_u32 v38, 4294938624, v38\n"
  "ds_read2st64_b32 v[128:129], v38 offset0:0 offset1:1\n"
  "ds_read2st64_b32 v[130:131], v38 offset0:2 offset1:3\n"
  "v_rcp_f32 v148, v148\n"
  "v_mul_f32 v78, v78, v150\n"
  "v_mfma_f32_32x32x16_bf16 v[112:127], a[212:215], v[136:139], v[112:127]\n"
  "v_mul_f32 v79, v79, v148\n"
  "v_cvt_pk_bf16_f32 v103, v78, v79\n"
  "buffer_store_dwordx4 v[96:99], v49, s[24:27], s42 offen sc1\n"
  "buffer_store_dwordx4 v[100:103], v49, s[24:27], s42 offen offset:512 sc1\n"
  "s_add_u32 s42, s42, 114688\n"
  "s_waitcnt lgkmcnt(2)\n"
  "v_mfma_f32_32x32x16_bf16 v[80:95], a[216:219], v[140:143], v[80:95]\n"
  "ds_read2st64_b32 v[132:133], v38 offset0:4 offset1:5\n"
  "ds_read2st64_b32 v[134:135], v38 offset0:6 offset1:7\n"
  "v_mfma_f32_32x32x16_bf16 v[112:127], a[220:223], v[140:143], v[112:127]\n"
  "v_add_u32 v23, 128, v23\n"
  "s_add_u32 s39, s23, 0\n"
  "s_add_u32 m0, s43, 0\n"
  "s_waitcnt lgkmcnt(2)\n"
  "v_mfma_f32_32x32x16_bf16 v[64:79], a[0:3], v[128:131], 0\n"
  "ds_read2st64_b32 v[136:137], v38 offset0:8 offset1:9\n"
  "ds_read2st64_b32 v[138:139], v38 offset0:10 offset1:11\n"
  "buffer_load_dwordx4 v23, s[24:27], s39 offen sc1 lds\n"
  "v_mfma_f32_32x32x16_bf16 v[96:111], a[4:7], v[128:131], 0\n"
  "v_xor_b32 v63, 0x8000, v34\n"
  "v_and_b32 v145, 0xffff0000, v34\n"
  "v_and_b32 v29, 0xffff, v63\n"
  "s_add_u32 s39, s23, 65536\n"
  "s_add_u32 m0, s43, 1024\n"
  "s_waitcnt lgkmcnt(2)\n"
  "v_mfma_f32_32x32x16_bf16 v[64:79], a[8:11], v[132:135], v[64:79]\n"
  "ds_read2st64_b32 v[140:141], v38 offset0:12 offset1:13\n"
  "ds_read2st64_b32 v[142:143], v38 offset0:14 offset1:15\n"
  "buffer_load_dwordx4 v23, s[24:27], s39 offen sc1 lds\n"
  "s_waitcnt vmcnt(8)\n"
  "ds_read_b64_tr_b16 v[152:153], v46 offset:16384\n"
  "ds_read_b64_tr_b16 v[154:155], v46 offset:17408\n"
  "v_mfma_f32_32x32x16_bf16 v[96:111], a[12:15], v[132:135], v[96:111]\n"
  "ds_read_b64_tr_b16 v[156:157], v47 offset:16384\n"
  "ds_read_b64_tr_b16 v[158:159], v47 offset:17408\n"
  "s_add_u32 s39, s23, 131072\n"
  "s_add_u32 m0, s43, 2048\n"
  "s_waitcnt lgkmcnt(6)\n"
  "v_mfma_f32_32x32x16_bf16 v[64:79], a[16:19], v[136:139], v[64:79]\n"
  "ds_read2st64_b32 v[128:129], v38 offset0:16 offset1:17\n"
  "ds_read2st64_b32 v[130:131], v38 offset0:18 offset1:19\n"
  "buffer_load_dwordx4 v23, s[24:27], s39 offen sc1 lds\n"
  "ds_read_b64_tr_b16 v[160:161], v50 offset:16384\n"
  "ds_read_b64_tr_b16 v[162:163], v50 offset:17408\n"
  "v_mfma_f32_32x32x16_bf16 v[96:111], a[20:23], v[136:139], v[96:111]\n"
  "v_add_u32 v7, 8192, v7\n"
  "ds_read_b64_tr_b16 v[164:165], v51 offset:16384\n"
  "ds_read_b64_tr_b16 v[166:167], v51 offset:17408\n"
  "s_add_u32 s39, s23, 196608\n"
  "s_add_u32 m0, s43, 3072\n"
  "s_waitcnt lgkmcnt(10)\n"
  "v_mfma_f32_32x32x16_bf16 v[64:79], a[24:27], v[140:143], v[64:79]\n"
  "ds_read2st64_b32 v[132:133], v38 offset0:20 offset1:21\n"
  "ds_read2st64_b32 v[134:135], v38 offset0:22 offset1:23\n"
  "buffer_load_dwordx4 v23, s[24:27], s39 offen sc1 lds\n"
  "buffer_load_dwordx2 v[32:33], v7, s[24:27], 0 offen sc1\n"
  "buffer_load_dwordx2 v[34:35], v7, s[24:27], 0 offen offset:256 sc1\n"
  "v_mfma_f32_32x32x16_bf16 v[96:111], a[28:31], v[140:143], v[96:111]\n"
  "v_rcp_f32 v144, v145\n"
  "v_lshl_or_b32 v28, v29, 16, v29\n"
  "v_mul_f32 v145, v145, v145\n"
  "v_mul_f32 v144, 0xbfb8aa3b, v144\n"
  "v_mfma_f32_32x32x16_bf16 v[80:95], v[18:21], v[28:31], v[80:95]\n"
  "v_mfma_f32_32x32x16_bf16 v[112:127], v[24:27], v[28:31], v[112:127]\n"
  "s_waitcnt lgkmcnt(6)\n"
  "v_mfma_f32_32x32x16_bf16 v[64:79], a[32:35], v[128:131], v[64:79]\n"
  "ds_read2st64_b32 v[136:137], v38 offset0:24 offset1:25\n"
  "ds_read2st64_b32 v[138:139], v38 offset0:26 offset1:27\n"
  "s_add_u32 s39, s23, 262144\n"
  "s_add_u32 m0, s43, 4096\n"
  "v_mfma_f32_32x32x16_bf16 v[96:111], a[36:39], v[128:131], v[96:111]\n"
  "buffer_load_dwordx4 v23, s[24:27], s39 offen sc1 lds\n"
  "s_nop 2\n"
  "v_mul_f32 v63, v144, v80\n"
  "v_mul_f32 v146, v144, v81\n"
  "v_exp_f32 v63, v63\n"
  "s_waitcnt lgkmcnt(2)\n"
  "v_mfma_f32_32x32x16_bf16 v[64:79], a[40:43], v[132:135], v[64:79]\n"
  "ds_read2st64_b32 v[140:141], v38 offset0:28 offset1:29\n"
  "ds_read2st64_b32 v[142:143], v38 offset0:30 offset1:31\n"
  "s_waitcnt vmcnt(9)\n"
  "ds_read_b64_tr_b16 v[168:169], v46 offset:0\n"
  "ds_read_b64_tr_b16 v[170:171], v46 offset:1024\n"
  "s_add_u32 s39, s23, 327680\n"
  "s_add_u32 m0, s43, 5120\n"
  "v_mfma_f32_32x32x16_bf16 v[96:111], a[44:47], v[132:135], v[96:111]\n"
  "buffer_load_dwordx4 v23, s[24:27], s39 offen sc1 lds\n"
  "ds_read_b64_tr_b16 v[172:173], v47 offset:0\n"
  "ds_read_b64_tr_b16 v[174:175], v47 offset:1024\n"
  "v_mul_f32 v80, v80, v112\n"
  "v_mul_f32 v147, v144, v82\n"
  "s_waitcnt lgkmcnt(6)\n"
  "v_mfma_f32_32x32x16_bf16 v[64:79], a[48:51], v[136:139], v[64:79]\n"
  "ds_read2st64_b32 v[128:129], v38 offset0:32 offset1:33\n"
  "ds_read2st64_b32 v[130:131], v38 offset0:34 offset1:35\n"
  "ds_read_b64_tr_b16 v[176:177], v50 offset:0\n"
  "ds_read_b64_tr_b16 v[178:179], v50 offset:1024\n"
  "s_add_u32 s39, s23, 393216\n"
  "s_add_u32 m0, s43, 6144\n"
  "v_mfma_f32_32x32x16_bf16 v[96:111], a[52:55], v[136:139], v[96:111]\n"
  "buffer_load_dwordx4 v23, s[24:27], s39 offen sc1 lds\n"
  "ds_read_b64_tr_b16 v[180:181], v51 offset:0\n"
  "ds_read_b64_tr_b16 v[182:183], v51 offset:1024\n"
  "v_exp_f32 v146, v146\n"
  "s_waitcnt lgkmcnt(10)\n"
  "v_mfma_f32_32x32x16_bf16 v[64:79], a[56:59], v[140:143], v[64:79]\n"
  "ds_read2st64_b32 v[132:133], v38 offset0:36 offset1:37\n"
  "ds_read2st64_b32 v[134:135], v38 offset0:38 offset1:39\n"
  "v_mul_f32 v81, v81, v113\n"
  "s_waitcnt lgkmcnt(15)\n"
  "s_mul_i32 s22, s61, 28672\n"
  "s_sub_u32 m0, s72, 0\n"
  "v_mfma_f32_32x32x16_bf16 v[96:111], a[60:63], v[140:143], v[96:111]\n"
  "buffer_load_dwordx4 v39, s[48:51], s22 offen lds\n"
  "v_fma_f32 v148, v63, v145, v145\n"
  "v_rcp_f32 v148, v148\n"
  "v_mul_f32 v63, v144, v83\n"
  "s_waitcnt lgkmcnt(6)\n"
  "v_mfma_f32_32x32x16_bf16 v[64:79], a[64:67], v[128:131], v[64:79]\n"
  "ds_read2st64_b32 v[136:137], v38 offset0:40 offset1:41\n"
  "ds_read2st64_b32 v[138:139], v38 offset0:42 offset1:43\n"
  "v_exp_f32 v147, v147\n"
  "v_mul_f32 v82, v82, v114\n"
  "v_fma_f32 v149, v146, v145, v145\n"
  "v_mfma_f32_32x32x16_bf16 v[96:111], a[68:71], v[128:131], v[96:111]\n"
  "v_rcp_f32 v149, v149\n"
  "v_mul_f32 v80, v80, v148\n"
  "v_mul_f32 v146, v144, v84\n"
  "s_sub_u32 m0, s72, -1024\n"
  "s_waitcnt lgkmcnt(2)\n"
  "v_mfma_f32_32x32x16_bf16 v[64:79], a[72:75], v[132:135], v[64:79]\n"
  "ds_read2st64_b32 v[140:141], v38 offset0:44 offset1:45\n"
  "ds_read2st64_b32 v[142:143], v38 offset0:46 offset1:47\n"
  "buffer_load_dwordx4 v40, s[48:51], s22 offen lds\n"
  "v_exp_f32 v63, v63\n"
  "v_mul_f32 v83, v83, v115\n"
  "v_mfma_f32_32x32x16_bf16 v[96:111], a[76:79], v[132:135], v[96:111]\n"
  "v_fma_f32 v150, v147, v145, v145\n"
  "v_rcp_f32 v150, v150\n"
  "v_mul_f32 v81, v81, v149\n"
  "s_sub_u32 m0, s72, -2048\n"
  "s_waitcnt lgkmcnt(2)\n"
  "v_mfma_f32_32x32x16_bf16 v[64:79], a[80:83], v[136:139], v[64:79]\n"
  "ds_read2st64_b32 v[128:129], v38 offset0:48 offset1:49\n"
  "ds_read2st64_b32 v[130:131], v38 offset0:50 offset1:51\n"
  "buffer_load_dwordx4 v41, s[48:51], s22 offen lds\n"
  "v_cvt_pk_bf16_f32 v112, v80, v81\n"
  "v_mul_f32 v147, v144, v85\n"
  "v_mfma_f32_32x32x16_bf16 v[96:111], a[84:87], v[136:139], v[96:111]\n"
  "v_exp_f32 v146, v146\n"
  "v_mul_f32 v84, v84, v116\n"
  "v_fma_f32 v148, v63, v145, v145\n"
  "s_sub_u32 m0, s72, -3072\n"
  "s_waitcnt lgkmcnt(2)\n"
  "v_mfma_f32_32x32x16_bf16 v[64:79], a[88:91], v[140:143], v[64:79]\n"
  "ds_read2st64_b32 v[132:133], v38 offset0:52 offset1:53\n"
  "ds_read2st64_b32 v[134:135], v38 offset0:54 offset1:55\n"
  "buffer_load_dwordx4 v42, s[48:51], s22 offen lds\n"
  "v_rcp_f32 v148, v148\n"
  "v_mul_f32 v82, v82, v150\n"
  "v_mfma_f32_32x32x16_bf16 v[96:111], a[92:95], v[140:143], v[96:111]\n"
  "v_mul_f32 v63, v144, v86\n"
  "v_exp_f32 v147, v147\n"
  "v_mul_f32 v85, v85, v117\n"
  "v_fma_f32 v149, v146, v145, v145\n"
  "s_waitcnt lgkmcnt(2)\n"
  "v_mfma_f32_32x32x16_bf16 v[64:79], a[96:99], v[128:131], v[64:79]\n"
  "ds_read2st64_b32 v[136:137], v38 offset0:56 offset1:57\n"
  "ds_read2st64_b32 v[138:139], v38 offset0:58 offset1:59\n"
  "s_waitcnt lgkmcnt(15)\n"
  "s_mul_i32 s22, s63, 28672\n"
  "s_sub_u32 m0, s72, 16384\n"
  "v_mfma_f32_32x32x16_bf16 v[96:111], a[100:103], v[128:131], v[96:111]\n"
  "buffer_load_dwordx4 v39, s[48:51], s22 offen lds\n"
  "v_rcp_f32 v149, v149\n"
  "v_mul_f32 v83, v83, v148\n"
  "v_cvt_pk_bf16_f32 v113, v82, v83\n"
  "s_waitcnt lgkmcnt(2)\n"
  "v_mfma_f32_32x32x16_bf16 v[64:79], a[104:107], v[132:135], v[64:79]\n"
  "ds_read2st64_b32 v[140:141], v38 offset0:60 offset1:61\n"
  "ds_read2st64_b32 v[142:143], v38 offset0:62 offset1:63\n"
  "v_mul_f32 v146, v144, v87\n"
  "v_exp_f32 v63, v63\n"
  "s_sub_u32 m0, s72, 15360\n"
  "v_mfma_f32_32x32x16_bf16 v[96:111], a[108:111], v[132:135], v[96:111]\n"
  "buffer_load_dwordx4 v40, s[48:51], s22 offen lds\n"
  "v_mul_f32 v86, v86, v118\n"
  "v_fma_f32 v150, v147, v145, v145\n"
  "v_rcp_f32 v150, v150\n"
  "s_waitcnt lgkmcnt(2)\n"
  "v_mfma_f32_32x32x16_bf16 v[64:79], a[112:115], v[136:139], v[64:79]\n"
  "ds_read2st64_b32 v[128:129], v38 offset0:64 offset1:65\n"
  "ds_read2st64_b32 v[130:131], v38 offset0:66 offset1:67\n"
  "v_mul_f32 v84, v84, v149\n"
  "v_mul_f32 v147, v144, v88\n"
  "s_sub_u32 m0, s72, 14336\n"
  "v_mfma_f32_32x32x16_bf16 v[96:111], a[116:119], v[136:139], v[96:111]\n"
  "buffer_load_dwordx4 v41, s[48:51], s22 offen lds\n"
  "v_exp_f32 v146, v146\n"
  "v_mul_f32 v87, v87, v119\n"
  "v_fma_f32 v148, v63, v145, v145\n"
  "s_waitcnt lgkmcnt(2)\n"
  "v_mfma_f32_32x32x16_bf16 v[64:79], a[120:123], v[140:143], v[64:79]\n"
  "ds_read2st64_b32 v[132:133], v38 offset0:68 offset1:69\n"
  "ds_read2st64_b32 v[134:135], v38 offset0:70 offset1:71\n"
  "v_rcp_f32 v148, v148\n"
  "v_mul_f32 v85, v85, v150\n"
  "s_sub_u32 m0, s72, 13312\n"
  "v_mfma_f32_32x32x16_bf16 v[96:111], a[124:127], v[140:143], v[96:111]\n"
  "buffer_load_dwordx4 v42, s[48:51], s22 offen lds\n"
  "v_cvt_pk_bf16_f32 v114, v84, v85\n"
  "v_mul_f32 v63, v144, v89\n"
  "v_exp_f32 v147, v147\n"
  "s_waitcnt lgkmcnt(2)\n"
  "v_mfma_f32_32x32x16_bf16 v[64:79], a[128:131], v[128:131], v[64:79]\n"
  "ds_read2st64_b32 v[136:137], v38 offset0:72 offset1:73\n"
  "ds_read2st64_b32 v[138:139], v38 offset0:74 offset1:75\n"
  "v_mul_f32 v88, v88, v120\n"
  "v_fma_f32 v149, v146, v145, v145\n"
  "v_rcp_f32 v149, v149\n"
  "v_mfma_f32_32x32x16_bf16 v[96:111], a[132:135], v[128:131], v[96:111]\n"
  "v_mul_f32 v86, v86, v148\n"
  "v_mul_f32 v146, v144, v90\n"
  "s_waitcnt lgkmcnt(15)\n"
  "s_mul_i32 s22, s64, 28672\n"
  "s_sub_u32 m0, s72, 32768\n"
  "s_waitcnt lgkmcnt(2)\n"
  "v_mfma_f32_32x32x16_bf16 v[64:79], a[136:139], v[132:135], v[64:79]\n"
  "ds_read2st64_b32 v[140:141], v38 offset0:76 offset1:77\n"
  "ds_read2st64_b32 v[142:143], v38 offset0:78 offset1:79\n"
  "buffer_load_dwordx4 v39, s[48:51], s22 offen lds\n"
  "v_exp_f32 v63, v63\n"
  "v_mul_f32 v89, v89, v121\n"
  "v_mfma_f32_32x32x16_bf16 v[96:111], a[140:143], v[132:135], v[96:111]\n"
  "v_fma_f32 v150, v147, v145, v145\n"
  "v_rcp_f32 v150, v150\n"
  "v_mul_f32 v87, v87, v149\n"
  "s_sub_u32 m0, s72, 31744\n"
  "s_waitcnt lgkmcnt(2)\n"
  "v_mfma_f32_32x32x16_bf16 v[64:79], a[144:147], v[136:139], v[64:79]\n"
  "ds_read2st64_b32 v[128:129], v38 offset0:80 offset1:81\n"
  "ds_read2st64_b32 v[130:131], v38 offset0:82 offset1:83\n"
  "buffer_load_dwordx4 v40, s[48:51], s22 offen lds\n"
  "v_cvt_pk_bf16_f32 v115, v86, v87\n"
  "v_mul_f32 v147, v144, v91\n"
  "v_mfma_f32_32x32x16_bf16 v[96:111], a[148:151], v[136:139], v[96:111]\n"
  "v_exp_f32 v146, v146\n"
  "v_mul_f32 v90, v90, v122\n"
  "v_fma_f32 v148, v63, v145, v145\n"
  "s_sub_u32 m0, s72, 30720\n"
  "s_waitcnt lgkmcnt(2)\n"
  "v_mfma_f32_32x32x16_bf16 v[64:79], a[152:155], v[140:143], v[64:79]\n"
  "ds_read2st64_b32 v[132:133], v38 offset0:84 offset1:85\n"
  "ds_read2st64_b32 v[134:135], v38 offset0:86 offset1:87\n"
  "buffer_load_dwordx4 v41, s[48:51], s22 offen lds\n"
  "s_waitcnt vmcnt(7)\n"
  "ds_read_b64_tr_b16 v[184:185], v46 offset:49152\n"
  "ds_read_b64_tr_b16 v[186:187], v46 offset:50176\n"
  "v_mfma_f32_32x32x16_bf16 v[96:111], a[156:159], v[140:143], v[96:111]\n"
  "ds_read_b64_tr_b16 v[188:189], v47 offset:49152\n"
  "ds_read_b64_tr_b16 v[190:191], v47 offset:50176\n"
  "v_rcp_f32 v148, v148\n"
  "s_sub_u32 m0, s72, 29696\n"
  "s_waitcnt lgkmcnt(6)\n"
  "v_mfma_f32_32x32x16_bf16 v[64:79], a[160:163], v[128:131], v[64:79]\n"
  "ds_read2st64_b32 v[136:137], v38 offset0:88 offset1:89\n"
  "ds_read2st64_b32 v[138:139], v38 offset0:90 offset1:91\n"
  "buffer_load_dwordx4 v42, s[48:51], s22 offen lds\n"
  "ds_read_b64_tr_b16 v[192:193], v50 offset:49152\n"
  "ds_read_b64_tr_b16 v[194:195], v50 offset:50176\n"
  "v_mul_f32 v88, v88, v150\n"
  "v_mfma_f32_32x32x16_bf16 v[96:111], a[164:167], v[128:131], v[96:111]\n"
  "ds_read_b64_tr_b16 v[196:197], v51 offset:49152\n"
  "ds_read_b64_tr_b16 v[198:199], v51 offset:50176\n"
  "v_mul_f32 v63, v144, v92\n"
  "v_exp_f32 v147, v147\n"
  "s_waitcnt lgkmcnt(10)\n"
  "v_mfma_f32_32x32x16_bf16 v[64:79], a[168:171], v[132:135], v[64:79]\n"
  "ds_read2st64_b32 v[140:141], v38 offset0:92 offset1:93\n"
  "ds_read2st64_b32 v[142:143], v38 offset0:94 offset1:95\n"
  "v_mul_f32 v91, v91, v123\n"
  "s_waitcnt lgkmcnt(15)\n"
  "s_mul_i32 s22, s65, 28672\n"
  "s_sub_u32 m0, s72, 49152\n"
  "v_mfma_f32_32x32x16_bf16 v[96:111], a[172:175], v[132:135], v[96:111]\n"
  "buffer_load_dwordx4 v39, s[48:51], s22 offen lds\n"
  "v_fma_f32 v149, v146, v145, v145\n"
  "v_rcp_f32 v149, v149\n"
  "v_mul_f32 v89, v89, v148\n"
  "s_waitcnt lgkmcnt(6)\n"
  "v_mfma_f32_32x32x16_bf16 v[64:79], a[176:179], v[136:139], v[64:79]\n"
  "ds_read2st64_b32 v[128:129], v38 offset0:96 offset1:97\n"
  "ds_read2st64_b32 v[130:131], v38 offset0:98 offset1:99\n"
  "s_waitcnt vmcnt(5)\n"
  "ds_read_b64_tr_b16 v[200:201], v46 offset:32768\n"
  "ds_read_b64_tr_b16 v[202:203], v46 offset:33792\n"
  "s_sub_u32 m0, s72, 48128\n"
  "v_mfma_f32_32x32x16_bf16 v[96:111], a[180:183], v[136:139], v[96:111]\n"
  "buffer_load_dwordx4 v40, s[48:51], s22 offen lds\n"
  "ds_read_b64_tr_b16 v[204:205], v47 offset:32768\n"
  "ds_read_b64_tr_b16 v[206:207], v47 offset:33792\n"
  "v_cvt_pk_bf16_f32 v116, v88, v89\n"
  "v_mul_f32 v146, v144, v93\n"
  "s_waitcnt lgkmcnt(6)\n"
  "v_mfma_f32_32x32x16_bf16 v[64:79], a[184:187], v[140:143], v[64:79]\n"
  "ds_read2st64_b32 v[132:133], v38 offset0:100 offset1:101\n"
  "ds_read2st64_b32 v[134:135], v38 offset0:102 offset1:103\n"
  "ds_read_b64_tr_b16 v[208:209], v50 offset:32768\n"
  "ds_read_b64_tr_b16 v[210:211], v50 offset:33792\n"
  "s_sub_u32 m0, s72, 47104\n"
  "v_mfma_f32_32x32x16_bf16 v[96:111], a[188:191], v[140:143], v[96:111]\n"
  "buffer_load_dwordx4 v41, s[48:51], s22 offen lds\n"
  "ds_read_b64_tr_b16 v[212:213], v51 offset:32768\n"
  "ds_read_b64_tr_b16 v[214:215], v51 offset:33792\n"
  "v_exp_f32 v63, v63\n"
  "s_waitcnt lgkmcnt(10)\n"
  "v_mfma_f32_32x32x16_bf16 v[64:79], a[192:195], v[128:131], v[64:79]\n"
  "ds_read2st64_b32 v[136:137], v38 offset0:104 offset1:105\n"
  "ds_read2st64_b32 v[138:139], v38 offset0:106 offset1:107\n"
  "s_waitcnt vmcnt(15)\n"
  "s_barrier\n"
  "v_mul_f32 v92, v92, v124\n"
  "v_fma_f32 v150, v147, v145, v145\n"
  "v_mfma_f32_32x32x16_bf16 v[96:111], a[196:199], v[128:131], v[96:111]\n"
  "v_rcp_f32 v150, v150\n"
  "v_mul_f32 v90, v90, v149\n"
  "v_mul_f32 v147, v144, v94\n"
  "s_sub_u32 m0, s72, 46080\n"
  "s_waitcnt lgkmcnt(6)\n"
  "v_mfma_f32_32x32x16_bf16 v[64:79], a[200:203], v[132:135], v[64:79]\n"
  "ds_read2st64_b32 v[140:141], v38 offset0:108 offset1:109\n"
  "ds_read2st64_b32 v[142:143], v38 offset0:110 offset1:111\n"
  "buffer_load_dwordx4 v42, s[48:51], s22 offen lds\n"
  "v_exp_f32 v146, v146\n"
  "v_mul_f32 v93, v93, v125\n"
  "v_mfma_f32_32x32x16_bf16 v[96:111], a[204:207], v[132:135], v[96:111]\n"
  "v_fma_f32 v148, v63, v145, v145\n"
  "v_rcp_f32 v148, v148\n"
  "v_mul_f32 v91, v91, v150\n"
  "v_cvt_pk_bf16_f32 v117, v90, v91\n"
  "s_waitcnt lgkmcnt(2)\n"
  "v_mfma_f32_32x32x16_bf16 v[64:79], a[208:211], v[136:139], v[64:79]\n"
  "v_add_u32 v38, 4294938624, v38\n"
  "ds_read2st64_b32 v[128:129], v38 offset0:0 offset1:1\n"
  "ds_read2st64_b32 v[130:131], v38 offset0:2 offset1:3\n"
  "v_mul_f32 v63, v144, v95\n"
  "v_exp_f32 v147, v147\n"
  "v_mfma_f32_32x32x16_bf16 v[96:111], a[212:215], v[136:139], v[96:111]\n"
  "v_mul_f32 v94, v94, v126\n"
  "v_fma_f32 v149, v146, v145, v145\n"
  "v_rcp_f32 v149, v149\n"
  "v_mul_f32 v92, v92, v148\n"
  "s_waitcnt lgkmcnt(2)\n"
  "v_mfma_f32_32x32x16_bf16 v[64:79], a[216:219], v[140:143], v[64:79]\n"
  "ds_read2st64_b32 v[132:133], v38 offset0:4 offset1:5\n"
  "ds_read2st64_b32 v[134:135], v38 offset0:6 offset1:7\n"
  "v_exp_f32 v63, v63\n"
  "v_mul_f32 v95, v95, v127\n"
  "v_fma_f32 v150, v147, v145, v145\n"
  "v_mfma_f32_32x32x16_bf16 v[96:111], a[220:223], v[140:143], v[96:111]\n"
  "v_rcp_f32 v150, v150\n"
  "v_mul_f32 v93, v93, v149\n"
  "v_cvt_pk_bf16_f32 v118, v92, v93\n"
  "v_fma_f32 v148, v63, v145, v145\n"
  "v_rcp_f32 v148, v148\n"
  "v_mul_f32 v94, v94, v150\n"
  "v_mul_f32 v95, v95, v148\n"
  "v_cvt_pk_bf16_f32 v119, v94, v95\n"
  "buffer_store_dwordx4 v[112:115], v49, s[24:27], s42 offen sc1\n"
  "buffer_store_dwordx4 v[116:119], v49, s[24:27], s42 offen offset:512 sc1\n"
  "s_add_u32 s42, s42, 114688\n"
  "s_mul_i32 s22, s66, 28672\n"
  "s_sub_u32 m0, s72, 65536\n"
  "s_waitcnt lgkmcnt(2)\n"
  "v_mfma_f32_32x32x16_bf16 v[80:95], a[0:3], v[128:131], 0\n"
  "ds_read2st64_b32 v[136:137], v38 offset0:8 offset1:9\n"
  "ds_read2st64_b32 v[138:139], v38 offset0:10 offset1:11\n"
  "buffer_load_dwordx4 v39, s[48:51], s22 offen lds\n"
  "s_waitcnt vmcnt(22)\n"
  "L29_%=:\n"
  "v_xor_b32 v10, s8, v33\n"
  "v_bitop3_b32 v10, v10, v35, s8 bitop3:0xf6\n"
  "v_cmp_ne_u32 vcc, 0, v10\n"
  "s_nop 0\n"
  "s_and_b64 vcc, exec, vcc\n"
  "s_cbranch_vccnz L30_%=\n"
  "L31_%=:\n"
  "v_mfma_f32_32x32x16_bf16 v[112:127], a[4:7], v[128:131], 0\n"
  "v_xor_b32 v63, 0x8000, v32\n"
  "v_and_b32 v145, 0xffff0000, v32\n"
  "v_and_b32 v29, 0xffff, v63\n"
  "s_sub_u32 m0, s72, 64512\n"
  "s_waitcnt lgkmcnt(2)\n"
  "v_mfma_f32_32x32x16_bf16 v[80:95], a[8:11], v[132:135], v[80:95]\n"
  "ds_read2st64_b32 v[140:141], v38 offset0:12 offset1:13\n"
  "ds_read2st64_b32 v[142:143], v38 offset0:14 offset1:15\n"
  "buffer_load_dwordx4 v40, s[48:51], s22 offen lds\n"
  "v_rcp_f32 v144, v145\n"
  "v_lshl_or_b32 v28, v29, 16, v29\n"
  "v_mfma_f32_32x32x16_bf16 v[112:127], a[12:15], v[132:135], v[112:127]\n"
  "v_mul_f32 v145, v145, v145\n"
  "v_mul_f32 v144, 0xbfb8aa3b, v144\n"
  "v_mfma_f32_32x32x16_bf16 v[64:79], v[18:21], v[28:31], v[64:79]\n"
  "v_mfma_f32_32x32x16_bf16 v[96:111], v[24:27], v[28:31], v[96:111]\n"
  "s_sub_u32 m0, s72, 63488\n"
  "s_waitcnt lgkmcnt(2)\n"
  "v_mfma_f32_32x32x16_bf16 v[80:95], a[16:19], v[136:139], v[80:95]\n"
  "ds_read2st64_b32 v[128:129], v38 offset0:16 offset1:17\n"
  "ds_read2st64_b32 v[130:131], v38 offset0:18 offset1:19\n"
  "buffer_load_dwordx4 v41, s[48:51], s22 offen lds\n"
  "s_waitcnt vmcnt(9)\n"
  "ds_read_b64_tr_b16 a[0:1], v46 offset:16384\n"
  "ds_read_b64_tr_b16 a[2:3], v46 offset:17408\n"
  "v_mfma_f32_32x32x16_bf16 v[112:127], a[20:23], v[136:139], v[112:127]\n"
  "ds_read_b64_tr_b16 a[4:5], v47 offset:16384\n"
  "ds_read_b64_tr_b16 a[6:7], v47 offset:17408\n"
  "v_mul_f32 v63, v144, v64\n"
  "v_mul_f32 v146, v144, v65\n"
  "s_sub_u32 m0, s72, 62464\n"
  "s_waitcnt lgkmcnt(6)\n"
  "v_mfma_f32_32x32x16_bf16 v[80:95], a[24:27], v[140:143], v[80:95]\n"
  "ds_read2st64_b32 v[132:133], v38 offset0:20 offset1:21\n"
  "ds_read2st64_b32 v[134:135], v38 offset0:22 offset1:23\n"
  "buffer_load_dwordx4 v42, s[48:51], s22 offen lds\n"
  "ds_read_b64_tr_b16 a[8:9], v50 offset:16384\n"
  "ds_read_b64_tr_b16 a[10:11], v50 offset:17408\n"
  "v_mfma_f32_32x32x16_bf16 v[112:127], a[28:31], v[140:143], v[112:127]\n"
  "ds_read_b64_tr_b16 a[12:13], v51 offset:16384\n"
  "ds_read_b64_tr_b16 a[14:15], v51 offset:17408\n"
  "s_mul_i32 s22, s67, 28672\n"
  "s_sub_u32 m0, s72, 81920\n"
  "s_waitcnt lgkmcnt(10)\n"
  "v_mfma_f32_32x32x16_bf16 v[80:95], a[32:35], v[128:131], v[80:95]\n"
  "ds_read2st64_b32 v[136:137], v38 offset0:24 offset1:25\n"
  "ds_read2st64_b32 v[138:139], v38 offset0:26 offset1:27\n"
  "buffer_load_dwordx4 v39, s[48:51], s22 offen lds\n"
  "v_exp_f32 v63, v63\n"
  "v_mul_f32 v64, v64, v96\n"
  "v_mfma_f32_32x32x16_bf16 v[112:127], a[36:39], v[128:131], v[112:127]\n"
  "v_mul_f32 v147, v144, v66\n"
  "v_exp_f32 v146, v146\n"
  "v_mul_f32 v65, v65, v97\n"
  "s_sub_u32 m0, s72, 80896\n"
  "s_waitcnt lgkmcnt(6)\n"
  "v_mfma_f32_32x32x16_bf16 v[80:95], a[40:43], v[132:135], v[80:95]\n"
  "ds_read2st64_b32 v[140:141], v38 offset0:28 offset1:29\n"
  "ds_read2st64_b32 v[142:143], v38 offset0:30 offset1:31\n"
  "buffer_load_dwordx4 v40, s[48:51], s22 offen lds\n"
  "v_fma_f32 v148, v63, v145, v145\n"
  "v_rcp_f32 v148, v148\n"
  "v_mfma_f32_32x32x16_bf16 v[112:127], a[44:47], v[132:135], v[112:127]\n"
  "v_mul_f32 v63, v144, v67\n"
  "v_exp_f32 v147, v147\n"
  "v_mul_f32 v66, v66, v98\n"
  "s_sub_u32 m0, s72, 79872\n"
  "s_waitcnt lgkmcnt(2)\n"
  "v_mfma_f32_32x32x16_bf16 v[80:95], a[48:51], v[136:139], v[80:95]\n"
  "ds_read2st64_b32 v[128:129], v38 offset0:32 offset1:33\n"
  "ds_read2st64_b32 v[130:131], v38 offset0:34 offset1:35\n"
  "buffer_load_dwordx4 v41, s[48:51], s22 offen lds\n"
  "s_waitcnt vmcnt(9)\n"
  "ds_read_b64_tr_b16 a[16:17], v46 offset:0\n"
  "ds_read_b64_tr_b16 a[18:19], v46 offset:1024\n"
  "v_mfma_f32_32x32x16_bf16 v[112:127], a[52:55], v[136:139], v[112:127]\n"
  "ds_read_b64_tr_b16 a[20:21], v47 offset:0\n"
  "ds_read_b64_tr_b16 a[22:23], v47 offset:1024\n"
  "v_fma_f32 v149, v146, v145, v145\n"
  "s_sub_u32 m0, s72, 78848\n"
  "s_waitcnt lgkmcnt(6)\n"
  "v_mfma_f32_32x32x16_bf16 v[80:95], a[56:59], v[140:143], v[80:95]\n"
  "ds_read2st64_b32 v[132:133], v38 offset0:36 offset1:37\n"
  "ds_read2st64_b32 v[134:135], v38 offset0:38 offset1:39\n"
  "buffer_load_dwordx4 v42, s[48:51], s22 offen lds\n"
  "ds_read_b64_tr_b16 a[24:25], v50 offset:0\n"
  "ds_read_b64_tr_b16 a[26:27], v50 offset:1024\n"
  "v_mfma_f32_32x32x16_bf16 v[112:127], a[60:63], v[140:143], v[112:127]\n"
  "ds_read_b64_tr_b16 a[28:29], v51 offset:0\n"
  "ds_read_b64_tr_b16 a[30:31], v51 offset:1024\n"
  "s_waitcnt lgkmcnt(15)\n"
  "s_mul_i32 s22, s68, 28672\n"
  "s_sub_u32 m0, s72, 0\n"
  "s_waitcnt lgkmcnt(10)\n"
  "v_mfma_f32_32x32x16_bf16 v[80:95], a[64:67], v[128:131], v[80:95]\n"
  "ds_read2st64_b32 v[136:137], v38 offset0:40 offset1:41\n"
  "ds_read2st64_b32 v[138:139], v38 offset0:42 offset1:43\n"
  "buffer_load_dwordx4 v39, s[48:51], s22 offen lds\n"
  "v_rcp_f32 v149, v149\n"
  "v_mul_f32 v64, v64, v148\n"
  "v_mfma_f32_32x32x16_bf16 v[112:127], a[68:71], v[128:131], v[112:127]\n"
  "v_mul_f32 v146, v144, v68\n"
  "v_exp_f32 v63, v63\n"
  "v_mul_f32 v67, v67, v99\n"
  "s_sub_u32 m0, s72, -1024\n"
  "s_waitcnt lgkmcnt(6)\n"
  "v_mfma_f32_32x32x16_bf16 v[80:95], a[72:75], v[132:135], v[80:95]\n"
  "ds_read2st64_b32 v[140:141], v38 offset0:44 offset1:45\n"
  "ds_read2st64_b32 v[142:143], v38 offset0:46 offset1:47\n"
  "buffer_load_dwordx4 v40, s[48:51], s22 offen lds\n"
  "v_fma_f32 v150, v147, v145, v145\n"
  "v_rcp_f32 v150, v150\n"
  "v_mfma_f32_32x32x16_bf16 v[112:127], a[76:79], v[132:135], v[112:127]\n"
  "v_mul_f32 v65, v65, v149\n"
  "v_cvt_pk_bf16_f32 v96, v64, v65\n"
  "v_mul_f32 v147, v144, v69\n"
  "s_sub_u32 m0, s72, -2048\n"
  "s_waitcnt lgkmcnt(2)\n"
  "v_mfma_f32_32x32x16_bf16 v[80:95], a[80:83], v[136:139], v[80:95]\n"
  "ds_read2st64_b32 v[128:129], v38 offset0:48 offset1:49\n"
  "ds_read2st64_b32 v[130:131], v38 offset0:50 offset1:51\n"
  "buffer_load_dwordx4 v41, s[48:51], s22 offen lds\n"
  "v_exp_f32 v146, v146\n"
  "v_mul_f32 v68, v68, v100\n"
  "v_mfma_f32_32x32x16_bf16 v[112:127], a[84:87], v[136:139], v[112:127]\n"
  "v_fma_f32 v148, v63, v145, v145\n"
  "v_rcp_f32 v148, v148\n"
  "v_mul_f32 v66, v66, v150\n"
  "s_sub_u32 m0, s72, -3072\n"
  "s_waitcnt lgkmcnt(2)\n"
  "v_mfma_f32_32x32x16_bf16 v[80:95], a[88:91], v[140:143], v[80:95]\n"
  "ds_read2st64_b32 v[132:133], v38 offset0:52 offset1:53\n"
  "ds_read2st64_b32 v[134:135], v38 offset0:54 offset1:55\n"
  "buffer_load_dwordx4 v42, s[48:51], s22 offen lds\n"
  "v_mul_f32 v63, v144, v70\n"
  "v_exp_f32 v147, v147\n"
  "v_mfma_f32_32x32x16_bf16 v[112:127], a[92:95], v[140:143], v[112:127]\n"
  "v_mul_f32 v69, v69, v101\n"
  "v_fma_f32 v149, v146, v145, v145\n"
  "s_waitcnt lgkmcnt(15)\n"
  "s_mul_i32 s22, s69, 28672\n"
  "s_sub_u32 m0, s72, 16384\n"
  "s_waitcnt lgkmcnt(2)\n"
  "v_mfma_f32_32x32x16_bf16 v[80:95], a[96:99], v[128:131], v[80:95]\n"
  "ds_read2st64_b32 v[136:137], v38 offset0:56 offset1:57\n"
  "ds_read2st64_b32 v[138:139], v38 offset0:58 offset1:59\n"
  "buffer_load_dwordx4 v39, s[48:51], s22 offen lds\n"
  "v_rcp_f32 v149, v149\n"
  "v_mul_f32 v67, v67, v148\n"
  "v_mfma_f32_32x32x16_bf16 v[112:127], a[100:103], v[128:131], v[112:127]\n"
  "v_cvt_pk_bf16_f32 v97, v66, v67\n"
  "v_mul_f32 v146, v144, v71\n"
  "v_exp_f32 v63, v63\n"
  "s_sub_u32 m0, s72, 15360\n"
  "s_waitcnt lgkmcnt(2)\n"
  "v_mfma_f32_32x32x16_bf16 v[80:95], a[104:107], v[132:135], v[80:95]\n"
  "ds_read2st64_b32 v[140:141], v38 offset0:60 offset1:61\n"
  "ds_read2st64_b32 v[142:143], v38 offset0:62 offset1:63\n"
  "buffer_load_dwordx4 v40, s[48:51], s22 offen lds\n"
  "v_mul_f32 v70, v70, v102\n"
  "v_fma_f32 v150, v147, v145, v145\n"
  "v_mfma_f32_32x32x16_bf16 v[112:127], a[108:111], v[132:135], v[112:127]\n"
  "v_rcp_f32 v150, v150\n"
  "v_mul_f32 v68, v68, v149\n"
  "v_mul_f32 v147, v144, v72\n"
  "s_sub_u32 m0, s72, 14336\n"
  "s_waitcnt lgkmcnt(2)\n"
  "v_mfma_f32_32x32x16_bf16 v[80:95], a[112:115], v[136:139], v[80:95]\n"
  "ds_read2st64_b32 v[128:129], v38 offset0:64 offset1:65\n"
  "ds_read2st64_b32 v[130:131], v38 offset0:66 offset1:67\n"
  "buffer_load_dwordx4 v41, s[48:51], s22 offen lds\n"
  "v_exp_f32 v146, v146\n"
  "v_mul_f32 v71, v71, v103\n"
  "v_mfma_f32_32x32x16_bf16 v[112:127], a[116:119], v[136:139], v[112:127]\n"
  "v_fma_f32 v148, v63, v145, v145\n"
  "v_rcp_f32 v148, v148\n"
  "v_mul_f32 v69, v69, v150\n"
  "s_sub_u32 m0, s72, 13312\n"
  "s_waitcnt lgkmcnt(2)\n"
  "v_mfma_f32_32x32x16_bf16 v[80:95], a[120:123], v[140:143], v[80:95]\n"
  "ds_read2st64_b32 v[132:133], v38 offset0:68 offset1:69\n"
  "ds_read2st64_b32 v[134:135], v38 offset0:70 offset1:71\n"
  "buffer_load_dwordx4 v42, s[48:51], s22 offen lds\n"
  "s_waitcnt vmcnt(12)\n"
  "ds_read_b64_tr_b16 a[32:33], v52 offset:49152\n"
  "ds_read_b64_tr_b16 a[34:35], v52 offset:50176\n"
  "v_mfma_f32_32x32x16_bf16 v[112:127], a[124:127], v[140:143], v[112:127]\n"
  "ds_read_b64_tr_b16 a[36:37], v53 offset:49152\n"
  "ds_read_b64_tr_b16 a[38:39], v53 offset:50176\n"
  "s_waitcnt lgkmcnt(15)\n"
  "s_mul_i32 s22, s70, 28672\n"
  "s_sub_u32 m0, s72, 32768\n"
  "s_waitcnt lgkmcnt(6)\n"
  "v_mfma_f32_32x32x16_bf16 v[80:95], a[128:131], v[128:131], v[80:95]\n"
  "ds_read2st64_b32 v[136:137], v38 offset0:72 offset1:73\n"
  "ds_read2st64_b32 v[138:139], v38 offset0:74 offset1:75\n"
  "buffer_load_dwordx4 v39, s[48:51], s22 offen lds\n"
  "ds_read_b64_tr_b16 a[40:41], v54 offset:49152\n"
  "ds_read_b64_tr_b16 a[42:43], v54 offset:50176\n"
  "v_cvt_pk_bf16_f32 v98, v68, v69\n"
  "v_mfma_f32_32x32x16_bf16 v[112:127], a[132:135], v[128:131], v[112:127]\n"
  "ds_read_b64_tr_b16 a[44:45], v55 offset:49152\n"
  "ds_read_b64_tr_b16 a[46:47], v55 offset:50176\n"
  "v_mul_f32 v63, v144, v73\n"
  "s_sub_u32 m0, s72, 31744\n"
  "s_waitcnt lgkmcnt(10)\n"
  "v_mfma_f32_32x32x16_bf16 v[80:95], a[136:139], v[132:135], v[80:95]\n"
  "ds_read2st64_b32 v[140:141], v38 offset0:76 offset1:77\n"
  "ds_read2st64_b32 v[142:143], v38 offset0:78 offset1:79\n"
  "buffer_load_dwordx4 v40, s[48:51], s22 offen lds\n"
  "s_waitcnt vmcnt(10)\n"
  "ds_read_b64_tr_b16 a[48:49], v52 offset:32768\n"
  "ds_read_b64_tr_b16 a[50:51], v52 offset:33792\n"
  "v_mfma_f32_32x32x16_bf16 v[112:127], a[140:143], v[132:135], v[112:127]\n"
  "ds_read_b64_tr_b16 a[52:53], v53 offset:32768\n"
  "ds_read_b64_tr_b16 a[54:55], v53 offset:33792\n"
  "v_exp_f32 v147, v147\n"
  "s_sub_u32 m0, s72, 30720\n"
  "s_waitcnt lgkmcnt(10)\n"
  "v_mfma_f32_32x32x16_bf16 v[80:95], a[144:147], v[136:139], v[80:95]\n"
  "ds_read2st64_b32 v[128:129], v38 offset0:80 offset1:81\n"
  "ds_read2st64_b32 v[130:131], v38 offset0:82 offset1:83\n"
  "buffer_load_dwordx4 v41, s[48:51], s22 offen lds\n"
  "ds_read_b64_tr_b16 a[56:57], v54 offset:32768\n"
  "ds_read_b64_tr_b16 a[58:59], v54 offset:33792\n"
  "v_mul_f32 v72, v72, v104\n"
  "v_mfma_f32_32x32x16_bf16 v[112:127], a[148:151], v[136:139], v[112:127]\n"
  "ds_read_b64_tr_b16 a[60:61], v55 offset:32768\n"
  "ds_read_b64_tr_b16 a[62:63], v55 offset:33792\n"
  "v_fma_f32 v149, v146, v145, v145\n"
  "s_sub_u32 m0, s72, 29696\n"
  "s_waitcnt lgkmcnt(10)\n"
  "v_mfma_f32_32x32x16_bf16 v[80:95], a[152:155], v[140:143], v[80:95]\n"
  "ds_read2st64_b32 v[132:133], v38 offset0:84 offset1:85\n"
  "ds_read2st64_b32 v[134:135], v38 offset0:86 offset1:87\n"
  "buffer_load_dwordx4 v42, s[48:51], s22 offen lds\n"
  "v_rcp_f32 v149, v149\n"
  "v_mul_f32 v70, v70, v148\n"
  "v_mfma_f32_32x32x16_bf16 v[112:127], a[156:159], v[140:143], v[112:127]\n"
  "v_mul_f32 v146, v144, v74\n"
  "s_waitcnt lgkmcnt(15)\n"
  "s_mul_i32 s22, s71, 28672\n"
  "s_sub_u32 m0, s72, 49152\n"
  "s_waitcnt lgkmcnt(6)\n"
  "v_mfma_f32_32x32x16_bf16 v[80:95], a[160:163], v[128:131], v[80:95]\n"
  "ds_read2st64_b32 v[136:137], v38 offset0:88 offset1:89\n"
  "ds_read2st64_b32 v[138:139], v38 offset0:90 offset1:91\n"
  "buffer_load_dwordx4 v39, s[48:51], s22 offen lds\n"
  "s_waitcnt vmcnt(9)\n"
  "ds_read_b64_tr_b16 a[64:65], v46 offset:49152\n"
  "ds_read_b64_tr_b16 a[66:67], v46 offset:50176\n"
  "v_mfma_f32_32x32x16_bf16 v[112:127], a[164:167], v[128:131], v[112:127]\n"
  "ds_read_b64_tr_b16 a[68:69], v47 offset:49152\n"
  "ds_read_b64_tr_b16 a[70:71], v47 offset:50176\n"
  "v_exp_f32 v63, v63\n"
  "s_sub_u32 m0, s72, 48128\n"
  "s_waitcnt lgkmcnt(6)\n"
  "v_mfma_f32_32x32x16_bf16 v[80:95], a[168:171], v[132:135], v[80:95]\n"
  "ds_read2st64_b32 v[140:141], v38 offset0:92 offset1:93\n"
  "ds_read2st64_b32 v[142:143], v38 offset0:94 offset1:95\n"
  "buffer_load_dwordx4 v40, s[48:51], s22 offen lds\n"
  "ds_read_b64_tr_b16 a[72:73], v50 offset:49152\n"
  "ds_read_b64_tr_b16 a[74:75], v50 offset:50176\n"
  "v_mul_f32 v73, v73, v105\n"
  "v_mfma_f32_32x32x16_bf16 v[112:127], a[172:175], v[132:135], v[112:127]\n"
  "ds_read_b64_tr_b16 a[76:77], v51 offset:49152\n"
  "ds_read_b64_tr_b16 a[78:79], v51 offset:50176\n"
  "v_fma_f32 v150, v147, v145, v145\n"
  "s_sub_u32 m0, s72, 47104\n"
  "s_waitcnt lgkmcnt(10)\n"
  "v_mfma_f32_32x32x16_bf16 v[80:95], a[176:179], v[136:139], v[80:95]\n"
  "ds_read2st64_b32 v[128:129], v38 offset0:96 offset1:97\n"
  "ds_read2st64_b32 v[130:131], v38 offset0:98 offset1:99\n"
  "buffer_load_dwordx4 v41, s[48:51], s22 offen lds\n"
  "v_rcp_f32 v150, v150\n"
  "v_mul_f32 v71, v71, v149\n"
  "v_mfma_f32_32x32x16_bf16 v[112:127], a[180:183], v[136:139], v[112:127]\n"
  "v_cvt_pk_bf16_f32 v99, v70, v71\n"
  "v_mul_f32 v147, v144, v75\n"
  "v_exp_f32 v146, v146\n"
  "s_sub_u32 m0, s72, 46080\n"
  "s_waitcnt lgkmcnt(6)\n"
  "v_mfma_f32_32x32x16_bf16 v[80:95], a[184:187], v[140:143], v[80:95]\n"
  "ds_read2st64_b32 v[132:133], v38 offset0:100 offset1:101\n"
  "ds_read2st64_b32 v[134:135], v38 offset0:102 offset1:103\n"
  "buffer_load_dwordx4 v42, s[48:51], s22 offen lds\n"
  "v_mul_f32 v74, v74, v106\n"
  "v_fma_f32 v148, v63, v145, v145\n"
  "v_mfma_f32_32x32x16_bf16 v[112:127], a[188:191], v[140:143], v[112:127]\n"
  "s_waitcnt vmcnt(8)\n"
  "ds_read_b64_tr_b16 a[80:81], v46 offset:32768\n"
  "ds_read_b64_tr_b16 a[82:83], v46 offset:33792\n"
  "s_lshl_b32 s53, s53, 10\n"
  "s_waitcnt lgkmcnt(4)\n"
  "v_mfma_f32_32x32x16_bf16 v[80:95], a[192:195], v[128:131], v[80:95]\n"
  "ds_read2st64_b32 v[136:137], v38 offset0:104 offset1:105\n"
  "ds_read2st64_b32 v[138:139], v38 offset0:106 offset1:107\n"
  "buffer_load_dwordx4 v[14:17], v56, s[24:27], s53 offen sc1\n"
  "ds_read_b64_tr_b16 a[84:85], v47 offset:32768\n"
  "ds_read_b64_tr_b16 a[86:87], v47 offset:33792\n"
  "v_mfma_f32_32x32x16_bf16 v[112:127], a[196:199], v[128:131], v[112:127]\n"
  "ds_read_b64_tr_b16 a[88:89], v50 offset:32768\n"
  "ds_read_b64_tr_b16 a[90:91], v50 offset:33792\n"
  "v_rcp_f32 v148, v148\n"
  "s_lshl_b32 s58, s58, 10\n"
  "s_waitcnt lgkmcnt(8)\n"
  "v_mfma_f32_32x32x16_bf16 v[80:95], a[200:203], v[132:135], v[80:95]\n"
  "ds_read2st64_b32 v[140:141], v38 offset0:108 offset1:109\n"
  "ds_read2st64_b32 v[142:143], v38 offset0:110 offset1:111\n"
  "buffer_load_dwordx4 v[216:219], v56, s[24:27], s58 offen sc1\n"
  "ds_read_b64_tr_b16 a[92:93], v51 offset:32768\n"
  "ds_read_b64_tr_b16 a[94:95], v51 offset:33792\n"
  "v_mul_f32 v72, v72, v150\n"
  "v_mfma_f32_32x32x16_bf16 v[112:127], a[204:207], v[132:135], v[112:127]\n"
  "v_mul_f32 v63, v144, v76\n"
  "v_exp_f32 v147, v147\n"
  "v_mul_f32 v75, v75, v107\n"
  "s_lshl_b32 s59, s59, 10\n"
  "s_waitcnt lgkmcnt(8)\n"
  "v_mfma_f32_32x32x16_bf16 v[80:95], a[208:211], v[136:139], v[80:95]\n"
  "buffer_load_dwordx4 v[220:223], v56, s[24:27], s59 offen sc1\n"
  "v_fma_f32 v149, v146, v145, v145\n"
  "v_rcp_f32 v149, v149\n"
  "v_mul_f32 v73, v73, v148\n"
  "v_cvt_pk_bf16_f32 v100, v72, v73\n"
  "v_mfma_f32_32x32x16_bf16 v[112:127], a[212:215], v[136:139], v[112:127]\n"
  "v_mul_f32 v146, v144, v77\n"
  "v_exp_f32 v63, v63\n"
  "v_mul_f32 v76, v76, v108\n"
  "s_lshl_b32 s60, s60, 10\n"
  "s_waitcnt lgkmcnt(2)\n"
  "v_mfma_f32_32x32x16_bf16 v[80:95], a[216:219], v[140:143], v[80:95]\n"
  "buffer_load_dwordx4 v[224:227], v56, s[24:27], s60 offen sc1\n"
  "v_fma_f32 v150, v147, v145, v145\n"
  "v_rcp_f32 v150, v150\n"
  "v_mul_f32 v74, v74, v149\n"
  "v_mul_f32 v147, v144, v78\n"
  "v_mfma_f32_32x32x16_bf16 v[112:127], a[220:223], v[140:143], v[112:127]\n"
  "v_exp_f32 v146, v146\n"
  "v_mul_f32 v77, v77, v109\n"
  "v_fma_f32 v148, v63, v145, v145\n"
  "v_rcp_f32 v148, v148\n"
  "v_mul_f32 v75, v75, v150\n"
  "v_cvt_pk_bf16_f32 v101, v74, v75\n"
  "v_mul_f32 v63, v144, v79\n"
  "v_exp_f32 v147, v147\n"
  "v_mul_f32 v78, v78, v110\n"
  "v_fma_f32 v149, v146, v145, v145\n"
  "v_rcp_f32 v149, v149\n"
  "v_mul_f32 v76, v76, v148\n"
  "v_exp_f32 v63, v63\n"
  "v_mul_f32 v79, v79, v111\n"
  "v_fma_f32 v150, v147, v145, v145\n"
  "v_rcp_f32 v150, v150\n"
  "v_mul_f32 v77, v77, v149\n"
  "v_cvt_pk_bf16_f32 v102, v76, v77\n"
  "v_fma_f32 v148, v63, v145, v145\n"
  "v_rcp_f32 v148, v148\n"
  "v_mul_f32 v78, v78, v150\n"
  "v_mul_f32 v79, v79, v148\n"
  "v_cvt_pk_bf16_f32 v103, v78, v79\n"
  "buffer_store_dwordx4 v[96:99], v49, s[24:27], s42 offen sc1\n"
  "buffer_store_dwordx4 v[100:103], v49, s[24:27], s42 offen offset:512 sc1\n"
  "s_add_u32 s42, s42, 114688\n"
  "v_xor_b32 v63, 0x8000, v34\n"
  "v_and_b32 v145, 0xffff0000, v34\n"
  "v_and_b32 v29, 0xffff, v63\n"
  "v_rcp_f32 v144, v145\n"
  "v_lshl_or_b32 v28, v29, 16, v29\n"
  "v_mul_f32 v145, v145, v145\n"
  "v_mul_f32 v144, 0xbfb8aa3b, v144\n"
  "v_mfma_f32_32x32x16_bf16 v[80:95], v[18:21], v[28:31], v[80:95]\n"
  "v_mfma_f32_32x32x16_bf16 v[112:127], v[24:27], v[28:31], v[112:127]\n"
  "s_lshl_b32 s61, s61, 10\n"
  "s_lshl_b32 s63, s63, 10\n"
  "s_lshl_b32 s64, s64, 10\n"
  "s_lshl_b32 s65, s65, 10\n"
  "s_lshl_b32 s66, s66, 10\n"
  "s_lshl_b32 s67, s67, 10\n"
  "s_lshl_b32 s68, s68, 10\n"
  "s_lshl_b32 s69, s69, 10\n"
  "s_lshl_b32 s70, s70, 10\n"
  "s_lshl_b32 s71, s71, 10\n"
  "buffer_load_dwordx4 v[38:41], v56, s[24:27], s61 offen sc1\n"
  "v_pk_mul_f32 v[128:129], v[80:81], v[144:145] op_sel_hi:[1,0]\n"
  "v_pk_mul_f32 v[80:81], v[80:81], v[112:113]\n"
  "v_exp_f32 v128, v128\n"
  "v_exp_f32 v129, v129\n"
  "v_pk_mul_f32 v[130:131], v[82:83], v[144:145] op_sel_hi:[1,0]\n"
  "v_pk_mul_f32 v[82:83], v[82:83], v[114:115]\n"
  "v_pk_fma_f32 v[132:133], v[128:129], v[144:145], v[144:145] op_sel:[0,1,1] op_sel_hi:[1,1,1]\n"
  "v_exp_f32 v130, v130\n"
  "v_exp_f32 v131, v131\n"
  "buffer_load_dwordx4 v[42:45], v56, s[24:27], s63 offen sc1\n"
  "v_rcp_f32 v22, v132\n"
  "v_rcp_f32 v23, v133\n"
  "v_pk_mul_f32 v[128:129], v[84:85], v[144:145] op_sel_hi:[1,0]\n"
  "v_pk_mul_f32 v[84:85], v[84:85], v[116:117]\n"
  "v_pk_fma_f32 v[134:135], v[130:131], v[144:145], v[144:145] op_sel:[0,1,1] op_sel_hi:[1,1,1]\n"
  "v_exp_f32 v128, v128\n"
  "v_exp_f32 v129, v129\n"
  "v_pk_mul_f32 v[80:81], v[80:81], v[22:23]\n"
  "v_cvt_pk_bf16_f32 v112, v80, v81\n"
  "buffer_load_dwordx4 v[64:67], v56, s[24:27], s64 offen sc1\n"
  "v_rcp_f32 v136, v134\n"
  "v_rcp_f32 v137, v135\n"
  "v_pk_mul_f32 v[130:131], v[86:87], v[144:145] op_sel_hi:[1,0]\n"
  "v_pk_mul_f32 v[86:87], v[86:87], v[118:119]\n"
  "v_pk_fma_f32 v[132:133], v[128:129], v[144:145], v[144:145] op_sel:[0,1,1] op_sel_hi:[1,1,1]\n"
  "v_exp_f32 v130, v130\n"
  "v_exp_f32 v131, v131\n"
  "v_pk_mul_f32 v[82:83], v[82:83], v[136:137]\n"
  "v_cvt_pk_bf16_f32 v113, v82, v83\n"
  "buffer_load_dwordx4 v[68:71], v56, s[24:27], s65 offen sc1\n"
  "v_rcp_f32 v22, v132\n"
  "v_rcp_f32 v23, v133\n"
  "v_pk_mul_f32 v[128:129], v[88:89], v[144:145] op_sel_hi:[1,0]\n"
  "v_pk_mul_f32 v[88:89], v[88:89], v[120:121]\n"
  "v_pk_fma_f32 v[134:135], v[130:131], v[144:145], v[144:145] op_sel:[0,1,1] op_sel_hi:[1,1,1]\n"
  "v_exp_f32 v128, v128\n"
  "v_exp_f32 v129, v129\n"
  "v_pk_mul_f32 v[84:85], v[84:85], v[22:23]\n"
  "v_cvt_pk_bf16_f32 v114, v84, v85\n"
  "buffer_load_dwordx4 v[72:75], v56, s[24:27], s66 offen sc1\n"
  "v_rcp_f32 v136, v134\n"
  "v_rcp_f32 v137, v135\n"
  "v_pk_mul_f32 v[130:131], v[90:91], v[144:145] op_sel_hi:[1,0]\n"
  "v_pk_mul_f32 v[90:91], v[90:91], v[122:123]\n"
  "v_pk_fma_f32 v[132:133], v[128:129], v[144:145], v[144:145] op_sel:[0,1,1] op_sel_hi:[1,1,1]\n"
  "v_exp_f32 v130, v130\n"
  "v_exp_f32 v131, v131\n"
  "v_pk_mul_f32 v[86:87], v[86:87], v[136:137]\n"
  "v_cvt_pk_bf16_f32 v115, v86, v87\n"
  "buffer_load_dwordx4 v[76:79], v56, s[24:27], s67 offen sc1\n"
  "v_rcp_f32 v22, v132\n"
  "v_rcp_f32 v23, v133\n"
  "v_pk_mul_f32 v[128:129], v[92:93], v[144:145] op_sel_hi:[1,0]\n"
  "v_pk_mul_f32 v[92:93], v[92:93], v[124:125]\n"
  "v_pk_fma_f32 v[134:135], v[130:131], v[144:145], v[144:145] op_sel:[0,1,1] op_sel_hi:[1,1,1]\n"
  "v_exp_f32 v128, v128\n"
  "v_exp_f32 v129, v129\n"
  "v_pk_mul_f32 v[88:89], v[88:89], v[22:23]\n"
  "v_cvt_pk_bf16_f32 v116, v88, v89\n"
  "buffer_load_dwordx4 v[96:99], v56, s[24:27], s68 offen sc1\n"
  "v_rcp_f32 v136, v134\n"
  "v_rcp_f32 v137, v135\n"
  "v_pk_mul_f32 v[130:131], v[94:95], v[144:145] op_sel_hi:[1,0]\n"
  "v_pk_mul_f32 v[94:95], v[94:95], v[126:127]\n"
  "v_pk_fma_f32 v[132:133], v[128:129], v[144:145], v[144:145] op_sel:[0,1,1] op_sel_hi:[1,1,1]\n"
  "v_exp_f32 v130, v130\n"
  "v_exp_f32 v131, v131\n"
  "v_pk_mul_f32 v[90:91], v[90:91], v[136:137]\n"
  "v_cvt_pk_bf16_f32 v117, v90, v91\n"
  "buffer_load_dwordx4 v[100:103], v56, s[24:27], s69 offen sc1\n"
  "v_rcp_f32 v22, v132\n"
  "v_rcp_f32 v23, v133\n"
  "v_pk_fma_f32 v[134:135], v[130:131], v[144:145], v[144:145] op_sel:[0,1,1] op_sel_hi:[1,1,1]\n"
  "v_pk_mul_f32 v[92:93], v[92:93], v[22:23]\n"
  "v_cvt_pk_bf16_f32 v118, v92, v93\n"
  "v_rcp_f32 v136, v134\n"
  "v_rcp_f32 v137, v135\n"
  "s_nop 0\n"
  "v_pk_mul_f32 v[94:95], v[94:95], v[136:137]\n"
  "v_cvt_pk_bf16_f32 v119, v94, v95\n"
  "buffer_store_dwordx4 v[112:115], v49, s[24:27], s42 offen sc1\n"
  "buffer_store_dwordx4 v[116:119], v49, s[24:27], s42 offen offset:512 sc1\n"
  "s_waitcnt vmcnt(20)\n"
  "ds_read_b64_tr_b16 a[96:97], v46 offset:16384\n"
  "ds_read_b64_tr_b16 a[98:99], v46 offset:17408\n"
  "ds_read_b64_tr_b16 a[100:101], v47 offset:16384\n"
  "ds_read_b64_tr_b16 a[102:103], v47 offset:17408\n"
  "ds_read_b64_tr_b16 a[104:105], v50 offset:16384\n"
  "ds_read_b64_tr_b16 a[106:107], v50 offset:17408\n"
  "ds_read_b64_tr_b16 a[108:109], v51 offset:16384\n"
  "ds_read_b64_tr_b16 a[110:111], v51 offset:17408\n"
  "s_waitcnt vmcnt(16)\n"
  "ds_read_b64_tr_b16 a[112:113], v46 offset:0\n"
  "ds_read_b64_tr_b16 a[114:115], v46 offset:1024\n"
  "ds_read_b64_tr_b16 a[116:117], v47 offset:0\n"
  "ds_read_b64_tr_b16 a[118:119], v47 offset:1024\n"
  "ds_read_b64_tr_b16 a[120:121], v50 offset:0\n"
  "ds_read_b64_tr_b16 a[122:123], v50 offset:1024\n"
  "ds_read_b64_tr_b16 a[124:125], v51 offset:0\n"
  "ds_read_b64_tr_b16 a[126:127], v51 offset:1024\n"
  "buffer_load_dwordx4 v[104:107], v56, s[24:27], s70 offen sc1\n"
  "buffer_load_dwordx4 v[108:111], v56, s[24:27], s71 offen sc1\n"
  "s_waitcnt vmcnt(17)\n"
  "v_mfma_f32_16x16x32_bf16 v[18:21], a[224:227], v[14:17], 0\n"
  "v_max3_u32 v7, v14, v15, 0\n"
  "v_mfma_f32_16x16x32_bf16 v[22:25], a[228:231], v[14:17], 0\n"
  "v_max3_u32 v7, v16, v17, v7\n"
  "v_mfma_f32_16x16x32_bf16 v[26:29], a[232:235], v[14:17], 0\n"
  "v_mfma_f32_16x16x32_bf16 v[30:33], a[236:239], v[14:17], 0\n"
  "v_add_u32 v56, 114688, v56\n"
  "buffer_load_dwordx4 v[14:17], v56, s[24:27], s53 offen sc1\n"
  "s_mov_b32 s4, 0\n"
  "buffer_load_dword v144, v57, s[24:27], s4 offen sc1\n"
  "s_waitcnt vmcnt(18)\n"
  "v_mfma_f32_16x16x32_bf16 v[18:21], a[240:243], v[216:219], v[18:21]\n"
  "v_max3_u32 v7, v216, v217, v7\n"
  "v_mfma_f32_16x16x32_bf16 v[22:25], a[244:247], v[216:219], v[22:25]\n"
  "v_max3_u32 v7, v218, v219, v7\n"
  "v_mfma_f32_16x16x32_bf16 v[26:29], a[248:251], v[216:219], v[26:29]\n"
  "v_mfma_f32_16x16x32_bf16 v[30:33], a[252:255], v[216:219], v[30:33]\n"
  "buffer_load_dwordx4 v[216:219], v56, s[24:27], s58 offen sc1\n"
  "s_mov_b32 s4, 8192\n"
  "buffer_load_dword v145, v57, s[24:27], s4 offen sc1\n"
  "s_waitcnt vmcnt(19)\n"
  "v_mfma_f32_16x16x32_bf16 v[18:21], v[152:155], v[220:223], v[18:21]\n"
  "v_max3_u32 v7, v220, v221, v7\n"
  "v_mfma_f32_16x16x32_bf16 v[22:25], v[156:159], v[220:223], v[22:25]\n"
  "v_max3_u32 v7, v222, v223, v7\n"
  "v_mfma_f32_16x16x32_bf16 v[26:29], v[160:163], v[220:223], v[26:29]\n"
  "v_mfma_f32_16x16x32_bf16 v[30:33], v[164:167], v[220:223], v[30:33]\n"
  "buffer_load_dwordx4 v[220:223], v56, s[24:27], s59 offen sc1\n"
  "s_mov_b32 s4, 128\n"
  "buffer_load_dword v146, v57, s[24:27], s4 offen sc1\n"
  "s_waitcnt vmcnt(20)\n"
  "v_mfma_f32_16x16x32_bf16 v[18:21], v[168:171], v[224:227], v[18:21]\n"
  "v_max3_u32 v7, v224, v225, v7\n"
  "v_mfma_f32_16x16x32_bf16 v[22:25], v[172:175], v[224:227], v[22:25]\n"
  "v_max3_u32 v7, v226, v227, v7\n"
  "v_mfma_f32_16x16x32_bf16 v[26:29], v[176:179], v[224:227], v[26:29]\n"
  "v_mfma_f32_16x16x32_bf16 v[30:33], v[180:183], v[224:227], v[30:33]\n"
  "buffer_load_dwordx4 v[224:227], v56, s[24:27], s60 offen sc1\n"
  "s_mov_b32 s4, 8320\n"
  "buffer_load_dword v147, v57, s[24:27], s4 offen sc1\n"
  "s_waitcnt vmcnt(19)\n"
  "v_mfma_f32_16x16x32_bf16 v[18:21], v[184:187], v[38:41], v[18:21]\n"
  "v_max3_u32 v7, v38, v39, v7\n"
  "v_mfma_f32_16x16x32_bf16 v[22:25], v[188:191], v[38:41], v[22:25]\n"
  "v_max3_u32 v7, v40, v41, v7\n"
  "v_mfma_f32_16x16x32_bf16 v[26:29], v[192:195], v[38:41], v[26:29]\n"
  "v_mfma_f32_16x16x32_bf16 v[30:33], v[196:199], v[38:41], v[30:33]\n"
  "buffer_load_dwordx4 v[38:41], v56, s[24:27], s61 offen sc1\n"
  "s_mov_b32 s4, 4096\n"
  "buffer_load_dword v148, v57, s[24:27], s4 offen sc1\n"
  "s_waitcnt vmcnt(20)\n"
  "v_mfma_f32_16x16x32_bf16 v[18:21], v[200:203], v[42:45], v[18:21]\n"
  "v_max3_u32 v7, v42, v43, v7\n"
  "v_mfma_f32_16x16x32_bf16 v[22:25], v[204:207], v[42:45], v[22:25]\n"
  "v_max3_u32 v7, v44, v45, v7\n"
  "v_mfma_f32_16x16x32_bf16 v[26:29], v[208:211], v[42:45], v[26:29]\n"
  "v_mfma_f32_16x16x32_bf16 v[30:33], v[212:215], v[42:45], v[30:33]\n"
  "buffer_load_dwordx4 v[42:45], v56, s[24:27], s63 offen sc1\n"
  "s_mov_b32 s4, 12288\n"
  "buffer_load_dword v149, v57, s[24:27], s4 offen sc1\n"
  "s_waitcnt vmcnt(21)\n"
  "v_mfma_f32_16x16x32_bf16 v[18:21], a[0:3], v[64:67], v[18:21]\n"
  "v_max3_u32 v7, v64, v65, v7\n"
  "v_mfma_f32_16x16x32_bf16 v[22:25], a[4:7], v[64:67], v[22:25]\n"
  "v_max3_u32 v7, v66, v67, v7\n"
  "v_mfma_f32_16x16x32_bf16 v[26:29], a[8:11], v[64:67], v[26:29]\n"
  "v_mfma_f32_16x16x32_bf16 v[30:33], a[12:15], v[64:67], v[30:33]\n"
  "buffer_load_dwordx4 v[64:67], v56, s[24:27], s64 offen sc1\n"
  "s_mov_b32 s4, 4224\n"
  "buffer_load_dword v150, v57, s[24:27], s4 offen sc1\n"
  "s_waitcnt vmcnt(22)\n"
  "v_mfma_f32_16x16x32_bf16 v[18:21], a[16:19], v[68:71], v[18:21]\n"
  "v_max3_u32 v7, v68, v69, v7\n"
  "v_mfma_f32_16x16x32_bf16 v[22:25], a[20:23], v[68:71], v[22:25]\n"
  "v_max3_u32 v7, v70, v71, v7\n"
  "v_mfma_f32_16x16x32_bf16 v[26:29], a[24:27], v[68:71], v[26:29]\n"
  "v_mfma_f32_16x16x32_bf16 v[30:33], a[28:31], v[68:71], v[30:33]\n"
  "buffer_load_dwordx4 v[68:71], v56, s[24:27], s65 offen sc1\n"
  "s_mov_b32 s4, 12416\n"
  "buffer_load_dword v151, v57, s[24:27], s4 offen sc1\n"
  "s_waitcnt vmcnt(23)\n"
  "v_mfma_f32_16x16x32_bf16 v[18:21], a[32:35], v[72:75], v[18:21]\n"
  "v_max3_u32 v7, v72, v73, v7\n"
  "v_mfma_f32_16x16x32_bf16 v[22:25], a[36:39], v[72:75], v[22:25]\n"
  "v_max3_u32 v7, v74, v75, v7\n"
  "v_mfma_f32_16x16x32_bf16 v[26:29], a[40:43], v[72:75], v[26:29]\n"
  "v_mfma_f32_16x16x32_bf16 v[30:33], a[44:47], v[72:75], v[30:33]\n"
  "buffer_load_dwordx4 v[72:75], v56, s[24:27], s66 offen sc1\n"
  "s_waitcnt vmcnt(23)\n"
  "v_mfma_f32_16x16x32_bf16 v[18:21], a[48:51], v[76:79], v[18:21]\n"
  "v_max3_u32 v7, v76, v77, v7\n"
  "v_mfma_f32_16x16x32_bf16 v[22:25], a[52:55], v[76:79], v[22:25]\n"
  "v_max3_u32 v7, v78, v79, v7\n"
  "v_mfma_f32_16x16x32_bf16 v[26:29], a[56:59], v[76:79], v[26:29]\n"
  "v_mfma_f32_16x16x32_bf16 v[30:33], a[60:63], v[76:79], v[30:33]\n"
  "buffer_load_dwordx4 v[76:79], v56, s[24:27], s67 offen sc1\n"
  "s_waitcnt vmcnt(23)\n"
  "v_mfma_f32_16x16x32_bf16 v[18:21], a[64:67], v[96:99], v[18:21]\n"
  "v_max3_u32 v7, v96, v97, v7\n"
  "v_mfma_f32_16x16x32_bf16 v[22:25], a[68:71], v[96:99], v[22:25]\n"
  "v_max3_u32 v7, v98, v99, v7\n"
  "v_mfma_f32_16x16x32_bf16 v[26:29], a[72:75], v[96:99], v[26:29]\n"
  "v_mfma_f32_16x16x32_bf16 v[30:33], a[76:79], v[96:99], v[30:33]\n"
  "buffer_load_dwordx4 v[96:99], v56, s[24:27], s68 offen sc1\n"
  "s_barrier\n"
  "s_waitcnt vmcnt(23)\n"
  "v_mfma_f32_16x16x32_bf16 v[18:21], a[80:83], v[100:103], v[18:21]\n"
  "v_max3_u32 v7, v100, v101, v7\n"
  "v_mfma_f32_16x16x32_bf16 v[22:25], a[84:87], v[100:103], v[22:25]\n"
  "v_max3_u32 v7, v102, v103, v7\n"
  "v_mfma_f32_16x16x32_bf16 v[26:29], a[88:91], v[100:103], v[26:29]\n"
  "v_mfma_f32_16x16x32_bf16 v[30:33], a[92:95], v[100:103], v[30:33]\n"
  "buffer_load_dwordx4 v[100:103], v56, s[24:27], s69 offen sc1\n"
  "s_waitcnt vmcnt(21) lgkmcnt(0)\n"
  "v_mfma_f32_16x16x32_bf16 v[18:21], a[96:99], v[104:107], v[18:21]\n"
  "v_max3_u32 v7, v104, v105, v7\n"
  "v_mfma_f32_16x16x32_bf16 v[22:25], a[100:103], v[104:107], v[22:25]\n"
  "v_max3_u32 v7, v106, v107, v7\n"
  "v_mfma_f32_16x16x32_bf16 v[26:29], a[104:107], v[104:107], v[26:29]\n"
  "v_mfma_f32_16x16x32_bf16 v[30:33], a[108:111], v[104:107], v[30:33]\n"
  "buffer_load_dwordx4 v[104:107], v56, s[24:27], s70 offen sc1\n"
  "s_waitcnt vmcnt(21)\n"
  "v_mfma_f32_16x16x32_bf16 v[18:21], a[112:115], v[108:111], v[18:21]\n"
  "v_max3_u32 v7, v108, v109, v7\n"
  "v_mfma_f32_16x16x32_bf16 v[22:25], a[116:119], v[108:111], v[22:25]\n"
  "v_max3_u32 v7, v110, v111, v7\n"
  "v_mfma_f32_16x16x32_bf16 v[26:29], a[120:123], v[108:111], v[26:29]\n"
  "v_mfma_f32_16x16x32_bf16 v[30:33], a[124:127], v[108:111], v[30:33]\n"
  "buffer_load_dwordx4 v[108:111], v56, s[24:27], s71 offen sc1\n"
  "v_cmp_eq_u32 vcc, -1, v7\n"
  "s_nop 0\n"
  "s_and_b64 vcc, exec, vcc\n"
  "s_cbranch_vccnz L32_%=\n"
  "L33_%=:\n"
  "s_waitcnt vmcnt(21)\n"
  "v_mfma_f32_16x16x32_bf16 v[80:83], a[224:227], v[14:17], 0\n"
  "v_max3_u32 v7, v14, v15, 0\n"
  "v_mfma_f32_16x16x32_bf16 v[84:87], a[228:231], v[14:17], 0\n"
  "v_max3_u32 v7, v16, v17, v7\n"
  "v_mfma_f32_16x16x32_bf16 v[88:91], a[232:235], v[14:17], 0\n"
  "v_mfma_f32_16x16x32_bf16 v[92:95], a[236:239], v[14:17], 0\n"
  "v_add_u32 v56, 114688, v56\n"
  "buffer_load_dwordx4 v[14:17], v56, s[24:27], s53 offen sc1\n"
  "s_waitcnt vmcnt(20)\n"
  "v_mfma_f32_16x16x32_bf16 v[80:83], a[240:243], v[216:219], v[80:83]\n"
  "v_max3_u32 v7, v216, v217, v7\n"
  "v_mfma_f32_16x16x32_bf16 v[84:87], a[244:247], v[216:219], v[84:87]\n"
  "v_max3_u32 v7, v218, v219, v7\n"
  "v_mfma_f32_16x16x32_bf16 v[88:91], a[248:251], v[216:219], v[88:91]\n"
  "v_mfma_f32_16x16x32_bf16 v[92:95], a[252:255], v[216:219], v[92:95]\n"
  "buffer_load_dwordx4 v[216:219], v56, s[24:27], s58 offen sc1\n"
  "ds_write_b128 v59, v[22:25] offset:1024\n"
  "ds_write_b128 v60, v[26:29] offset:2048\n"
  "ds_write_b128 v61, v[30:33] offset:3072\n"
  "s_waitcnt vmcnt(19)\n"
  "v_mfma_f32_16x16x32_bf16 v[80:83], v[152:155], v[220:223], v[80:83]\n"
  "v_max3_u32 v7, v220, v221, v7\n"
  "v_mfma_f32_16x16x32_bf16 v[84:87], v[156:159], v[220:223], v[84:87]\n"
  "v_max3_u32 v7, v222, v223, v7\n"
  "v_mfma_f32_16x16x32_bf16 v[88:91], v[160:163], v[220:223], v[88:91]\n"
  "v_mfma_f32_16x16x32_bf16 v[92:95], v[164:167], v[220:223], v[92:95]\n"
  "buffer_load_dwordx4 v[220:223], v56, s[24:27], s59 offen sc1\n"
  "s_waitcnt vmcnt(18)\n"
  "v_mfma_f32_16x16x32_bf16 v[80:83], v[168:171], v[224:227], v[80:83]\n"
  "v_max3_u32 v7, v224, v225, v7\n"
  "v_mfma_f32_16x16x32_bf16 v[84:87], v[172:175], v[224:227], v[84:87]\n"
  "v_max3_u32 v7, v226, v227, v7\n"
  "v_mfma_f32_16x16x32_bf16 v[88:91], v[176:179], v[224:227], v[88:91]\n"
  "v_mfma_f32_16x16x32_bf16 v[92:95], v[180:183], v[224:227], v[92:95]\n"
  "buffer_load_dwordx4 v[224:227], v56, s[24:27], s60 offen sc1\n"
  "s_waitcnt lgkmcnt(0)\n"
  "s_barrier\n"
  "ds_read_b128 v[228:231], v62 offset:1024\n"
  "ds_read_b128 v[232:235], v62 offset:2048\n"
  "ds_read_b128 v[236:239], v62 offset:3072\n"
  "s_waitcnt vmcnt(17)\n"
  "v_mfma_f32_16x16x32_bf16 v[80:83], v[184:187], v[38:41], v[80:83]\n"
  "v_max3_u32 v7, v38, v39, v7\n"
  "v_mfma_f32_16x16x32_bf16 v[84:87], v[188:191], v[38:41], v[84:87]\n"
  "v_max3_u32 v7, v40, v41, v7\n"
  "v_mfma_f32_16x16x32_bf16 v[88:91], v[192:195], v[38:41], v[88:91]\n"
  "v_mfma_f32_16x16x32_bf16 v[92:95], v[196:199], v[38:41], v[92:95]\n"
  "buffer_load_dwordx4 v[38:41], v56, s[24:27], s61 offen sc1\n"
  "s_waitcnt vmcnt(16)\n"
  "v_mfma_f32_16x16x32_bf16 v[80:83], v[200:203], v[42:45], v[80:83]\n"
  "v_max3_u32 v7, v42, v43, v7\n"
  "v_mfma_f32_16x16x32_bf16 v[84:87], v[204:207], v[42:45], v[84:87]\n"
  "v_max3_u32 v7, v44, v45, v7\n"
  "v_mfma_f32_16x16x32_bf16 v[88:91], v[208:211], v[42:45], v[88:91]\n"
  "v_mfma_f32_16x16x32_bf16 v[92:95], v[212:215], v[42:45], v[92:95]\n"
  "buffer_load_dwordx4 v[42:45], v56, s[24:27], s63 offen sc1\n"
  "s_waitcnt vmcnt(12) lgkmcnt(0)\n"
  "s_waitcnt vmcnt(15)\n"
  "v_mfma_f32_16x16x32_bf16 v[80:83], a[0:3], v[64:67], v[80:83]\n"
  "v_max3_u32 v7, v64, v65, v7\n"
  "v_add_f32 v18, v18, v228\n"
  "v_mfma_f32_16x16x32_bf16 v[84:87], a[4:7], v[64:67], v[84:87]\n"
  "v_max3_u32 v7, v66, v67, v7\n"
  "v_add_f32 v19, v19, v229\n"
  "v_mfma_f32_16x16x32_bf16 v[88:91], a[8:11], v[64:67], v[88:91]\n"
  "v_add_f32 v20, v20, v230\n"
  "v_add_f32 v21, v21, v231\n"
  "v_mfma_f32_16x16x32_bf16 v[92:95], a[12:15], v[64:67], v[92:95]\n"
  "v_add_f32 v18, v18, v232\n"
  "v_add_f32 v19, v19, v233\n"
  "buffer_load_dwordx4 v[64:67], v56, s[24:27], s64 offen sc1\n"
  "s_waitcnt vmcnt(14)\n"
  "v_mfma_f32_16x16x32_bf16 v[80:83], a[16:19], v[68:71], v[80:83]\n"
  "v_max3_u32 v7, v68, v69, v7\n"
  "v_add_f32 v20, v20, v234\n"
  "v_mfma_f32_16x16x32_bf16 v[84:87], a[20:23], v[68:71], v[84:87]\n"
  "v_max3_u32 v7, v70, v71, v7\n"
  "v_add_f32 v21, v21, v235\n"
  "v_mfma_f32_16x16x32_bf16 v[88:91], a[24:27], v[68:71], v[88:91]\n"
  "v_add_f32 v18, v18, v236\n"
  "v_add_f32 v19, v19, v237\n"
  "v_mfma_f32_16x16x32_bf16 v[92:95], a[28:31], v[68:71], v[92:95]\n"
  "v_add_f32 v20, v20, v238\n"
  "v_add_f32 v21, v21, v239\n"
  "buffer_load_dwordx4 v[68:71], v56, s[24:27], s65 offen sc1\n"
  "s_waitcnt vmcnt(13)\n"
  "v_mfma_f32_16x16x32_bf16 v[80:83], a[32:35], v[72:75], v[80:83]\n"
  "v_max3_u32 v7, v72, v73, v7\n"
  "v_lshlrev_b32 v10, 16, v144\n"
  "v_mfma_f32_16x16x32_bf16 v[84:87], a[36:39], v[72:75], v[84:87]\n"
  "v_max3_u32 v7, v74, v75, v7\n"
  "v_and_b32 v144, 0xffff0000, v144\n"
  "v_mfma_f32_16x16x32_bf16 v[88:91], a[40:43], v[72:75], v[88:91]\n"
  "v_add_f32 v18, v18, v10\n"
  "v_add_f32 v19, v19, v144\n"
  "v_mfma_f32_16x16x32_bf16 v[92:95], a[44:47], v[72:75], v[92:95]\n"
  "v_lshlrev_b32 v10, 16, v145\n"
  "v_and_b32 v145, 0xffff0000, v145\n"
  "buffer_load_dwordx4 v[72:75], v56, s[24:27], s66 offen sc1\n"
  "s_waitcnt vmcnt(13)\n"
  "v_mfma_f32_16x16x32_bf16 v[80:83], a[48:51], v[76:79], v[80:83]\n"
  "v_max3_u32 v7, v76, v77, v7\n"
  "v_add_f32 v20, v20, v10\n"
  "v_mfma_f32_16x16x32_bf16 v[84:87], a[52:55], v[76:79], v[84:87]\n"
  "v_max3_u32 v7, v78, v79, v7\n"
  "v_add_f32 v21, v21, v145\n"
  "v_mfma_f32_16x16x32_bf16 v[88:91], a[56:59], v[76:79], v[88:91]\n"
  "v_cvt_pk_bf16_f32 v144, v18, v19\n"
  "v_cvt_pk_bf16_f32 v145, v20, v21\n"
  "v_mfma_f32_16x16x32_bf16 v[92:95], a[60:63], v[76:79], v[92:95]\n"
  "global_store_dwordx2 v58, v[144:145], s[56:57]\n"
  "v_add_u32 v58, 28672, v58\n"
  "buffer_load_dwordx4 v[76:79], v56, s[24:27], s67 offen sc1\n"
  "s_waitcnt vmcnt(14)\n"
  "v_mfma_f32_16x16x32_bf16 v[80:83], a[64:67], v[96:99], v[80:83]\n"
  "v_max3_u32 v7, v96, v97, v7\n"
  "v_mfma_f32_16x16x32_bf16 v[84:87], a[68:71], v[96:99], v[84:87]\n"
  "v_max3_u32 v7, v98, v99, v7\n"
  "v_mfma_f32_16x16x32_bf16 v[88:91], a[72:75], v[96:99], v[88:91]\n"
  "v_mfma_f32_16x16x32_bf16 v[92:95], a[76:79], v[96:99], v[92:95]\n"
  "buffer_load_dwordx4 v[96:99], v56, s[24:27], s68 offen sc1\n"
  "s_waitcnt vmcnt(14)\n"
  "v_mfma_f32_16x16x32_bf16 v[80:83], a[80:83], v[100:103], v[80:83]\n"
  "v_max3_u32 v7, v100, v101, v7\n"
  "v_mfma_f32_16x16x32_bf16 v[84:87], a[84:87], v[100:103], v[84:87]\n"
  "v_max3_u32 v7, v102, v103, v7\n"
  "v_mfma_f32_16x16x32_bf16 v[88:91], a[88:91], v[100:103], v[88:91]\n"
  "v_mfma_f32_16x16x32_bf16 v[92:95], a[92:95], v[100:103], v[92:95]\n"
  "buffer_load_dwordx4 v[100:103], v56, s[24:27], s69 offen sc1\n"
  "s_waitcnt vmcnt(14)\n"
  "v_mfma_f32_16x16x32_bf16 v[80:83], a[96:99], v[104:107], v[80:83]\n"
  "v_max3_u32 v7, v104, v105, v7\n"
  "v_mfma_f32_16x16x32_bf16 v[84:87], a[100:103], v[104:107], v[84:87]\n"
  "v_max3_u32 v7, v106, v107, v7\n"
  "v_mfma_f32_16x16x32_bf16 v[88:91], a[104:107], v[104:107], v[88:91]\n"
  "v_mfma_f32_16x16x32_bf16 v[92:95], a[108:111], v[104:107], v[92:95]\n"
  "buffer_load_dwordx4 v[104:107], v56, s[24:27], s70 offen sc1\n"
  "s_waitcnt vmcnt(14)\n"
  "v_mfma_f32_16x16x32_bf16 v[80:83], a[112:115], v[108:111], v[80:83]\n"
  "v_max3_u32 v7, v108, v109, v7\n"
  "v_mfma_f32_16x16x32_bf16 v[84:87], a[116:119], v[108:111], v[84:87]\n"
  "v_max3_u32 v7, v110, v111, v7\n"
  "v_mfma_f32_16x16x32_bf16 v[88:91], a[120:123], v[108:111], v[88:91]\n"
  "v_mfma_f32_16x16x32_bf16 v[92:95], a[124:127], v[108:111], v[92:95]\n"
  "buffer_load_dwordx4 v[108:111], v56, s[24:27], s71 offen sc1\n"
  "v_cmp_eq_u32 vcc, -1, v7\n"
  "s_nop 0\n"
  "s_and_b64 vcc, exec, vcc\n"
  "s_cbranch_vccnz L36_%=\n"
  "L37_%=:\n"
  "s_waitcnt vmcnt(14)\n"
  "v_mfma_f32_16x16x32_bf16 v[112:115], a[224:227], v[14:17], 0\n"
  "v_max3_u32 v7, v14, v15, 0\n"
  "v_mfma_f32_16x16x32_bf16 v[116:119], a[228:231], v[14:17], 0\n"
  "v_max3_u32 v7, v16, v17, v7\n"
  "v_mfma_f32_16x16x32_bf16 v[120:123], a[232:235], v[14:17], 0\n"
  "v_mfma_f32_16x16x32_bf16 v[124:127], a[236:239], v[14:17], 0\n"
  "v_add_u32 v56, 114688, v56\n"
  "buffer_load_dwordx4 v[14:17], v56, s[24:27], s53 offen sc1\n"
  "s_waitcnt vmcnt(14)\n"
  "v_mfma_f32_16x16x32_bf16 v[112:115], a[240:243], v[216:219], v[112:115]\n"
  "v_max3_u32 v7, v216, v217, v7\n"
  "v_mfma_f32_16x16x32_bf16 v[116:119], a[244:247], v[216:219], v[116:119]\n"
  "v_max3_u32 v7, v218, v219, v7\n"
  "v_mfma_f32_16x16x32_bf16 v[120:123], a[248:251], v[216:219], v[120:123]\n"
  "v_mfma_f32_16x16x32_bf16 v[124:127], a[252:255], v[216:219], v[124:127]\n"
  "buffer_load_dwordx4 v[216:219], v56, s[24:27], s58 offen sc1\n"
  "ds_write_b128 v59, v[84:87] offset:17408\n"
  "ds_write_b128 v60, v[88:91] offset:18432\n"
  "ds_write_b128 v61, v[92:95] offset:19456\n"
  "s_waitcnt vmcnt(14)\n"
  "v_mfma_f32_16x16x32_bf16 v[112:115], v[152:155], v[220:223], v[112:115]\n"
  "v_max3_u32 v7, v220, v221, v7\n"
  "v_mfma_f32_16x16x32_bf16 v[116:119], v[156:159], v[220:223], v[116:119]\n"
  "v_max3_u32 v7, v222, v223, v7\n"
  "v_mfma_f32_16x16x32_bf16 v[120:123], v[160:163], v[220:223], v[120:123]\n"
  "v_mfma_f32_16x16x32_bf16 v[124:127], v[164:167], v[220:223], v[124:127]\n"
  "buffer_load_dwordx4 v[220:223], v56, s[24:27], s59 offen sc1\n"
  "s_waitcnt vmcnt(14)\n"
  "v_mfma_f32_16x16x32_bf16 v[112:115], v[168:171], v[224:227], v[112:115]\n"
  "v_max3_u32 v7, v224, v225, v7\n"
  "v_mfma_f32_16x16x32_bf16 v[116:119], v[172:175], v[224:227], v[116:119]\n"
  "v_max3_u32 v7, v226, v227, v7\n"
  "v_mfma_f32_16x16x32_bf16 v[120:123], v[176:179], v[224:227], v[120:123]\n"
  "v_mfma_f32_16x16x32_bf16 v[124:127], v[180:183], v[224:227], v[124:127]\n"
  "buffer_load_dwordx4 v[224:227], v56, s[24:27], s60 offen sc1\n"
  "s_waitcnt lgkmcnt(0)\n"
  "s_barrier\n"
  "ds_read_b128 v[228:231], v62 offset:17408\n"
  "ds_read_b128 v[232:235], v62 offset:18432\n"
  "ds_read_b128 v[236:239], v62 offset:19456\n"
  "s_waitcnt vmcnt(14)\n"
  "v_mfma_f32_16x16x32_bf16 v[112:115], v[184:187], v[38:41], v[112:115]\n"
  "v_max3_u32 v7, v38, v39, v7\n"
  "v_mfma_f32_16x16x32_bf16 v[116:119], v[188:191], v[38:41], v[116:119]\n"
  "v_max3_u32 v7, v40, v41, v7\n"
  "v_mfma_f32_16x16x32_bf16 v[120:123], v[192:195], v[38:41], v[120:123]\n"
  "v_mfma_f32_16x16x32_bf16 v[124:127], v[196:199], v[38:41], v[124:127]\n"
  "buffer_load_dwordx4 v[38:41], v56, s[24:27], s61 offen sc1\n"
  "s_waitcnt vmcnt(14)\n"
  "v_mfma_f32_16x16x32_bf16 v[112:115], v[200:203], v[42:45], v[112:115]\n"
  "v_max3_u32 v7, v42, v43, v7\n"
  "v_mfma_f32_16x16x32_bf16 v[116:119], v[204:207], v[42:45], v[116:119]\n"
  "v_max3_u32 v7, v44, v45, v7\n"
  "v_mfma_f32_16x16x32_bf16 v[120:123], v[208:211], v[42:45], v[120:123]\n"
  "v_mfma_f32_16x16x32_bf16 v[124:127], v[212:215], v[42:45], v[124:127]\n"
  "buffer_load_dwordx4 v[42:45], v56, s[24:27], s63 offen sc1\n"
  "s_waitcnt vmcnt(27) lgkmcnt(0)\n"
  "s_waitcnt vmcnt(14)\n"
  "v_mfma_f32_16x16x32_bf16 v[112:115], a[0:3], v[64:67], v[112:115]\n"
  "v_max3_u32 v7, v64, v65, v7\n"
  "v_add_f32 v80, v80, v228\n"
  "v_mfma_f32_16x16x32_bf16 v[116:119], a[4:7], v[64:67], v[116:119]\n"
  "v_max3_u32 v7, v66, v67, v7\n"
  "v_add_f32 v81, v81, v229\n"
  "v_mfma_f32_16x16x32_bf16 v[120:123], a[8:11], v[64:67], v[120:123]\n"
  "v_add_f32 v82, v82, v230\n"
  "v_add_f32 v83, v83, v231\n"
  "v_mfma_f32_16x16x32_bf16 v[124:127], a[12:15], v[64:67], v[124:127]\n"
  "v_add_f32 v80, v80, v232\n"
  "v_add_f32 v81, v81, v233\n"
  "buffer_load_dwordx4 v[64:67], v56, s[24:27], s64 offen sc1\n"
  "s_waitcnt vmcnt(14)\n"
  "v_mfma_f32_16x16x32_bf16 v[112:115], a[16:19], v[68:71], v[112:115]\n"
  "v_max3_u32 v7, v68, v69, v7\n"
  "v_add_f32 v82, v82, v234\n"
  "v_mfma_f32_16x16x32_bf16 v[116:119], a[20:23], v[68:71], v[116:119]\n"
  "v_max3_u32 v7, v70, v71, v7\n"
  "v_add_f32 v83, v83, v235\n"
  "v_mfma_f32_16x16x32_bf16 v[120:123], a[24:27], v[68:71], v[120:123]\n"
  "v_add_f32 v80, v80, v236\n"
  "v_add_f32 v81, v81, v237\n"
  "v_mfma_f32_16x16x32_bf16 v[124:127], a[28:31], v[68:71], v[124:127]\n"
  "v_add_f32 v82, v82, v238\n"
  "v_add_f32 v83, v83, v239\n"
  "buffer_load_dwordx4 v[68:71], v56, s[24:27], s65 offen sc1\n"
  "s_waitcnt vmcnt(14)\n"
  "v_mfma_f32_16x16x32_bf16 v[112:115], a[32:35], v[72:75], v[112:115]\n"
  "v_max3_u32 v7, v72, v73, v7\n"
  "v_lshlrev_b32 v10, 16, v146\n"
  "v_mfma_f32_16x16x32_bf16 v[116:119], a[36:39], v[72:75], v[116:119]\n"
  "v_max3_u32 v7, v74, v75, v7\n"
  "v_and_b32 v146, 0xffff0000, v146\n"
  "v_mfma_f32_16x16x32_bf16 v[120:123], a[40:43], v[72:75], v[120:123]\n"
  "v_add_f32 v80, v80, v10\n"
  "v_add_f32 v81, v81, v146\n"
  "v_mfma_f32_16x16x32_bf16 v[124:127], a[44:47], v[72:75], v[124:127]\n"
  "v_lshlrev_b32 v10, 16, v147\n"
  "v_and_b32 v147, 0xffff0000, v147\n"
  "buffer_load_dwordx4 v[72:75], v56, s[24:27], s66 offen sc1\n"
  "s_waitcnt vmcnt(13)\n"
  "v_mfma_f32_16x16x32_bf16 v[112:115], a[48:51], v[76:79], v[112:115]\n"
  "v_max3_u32 v7, v76, v77, v7\n"
  "v_add_f32 v82, v82, v10\n"
  "v_mfma_f32_16x16x32_bf16 v[116:119], a[52:55], v[76:79], v[116:119]\n"
  "v_max3_u32 v7, v78, v79, v7\n"
  "v_add_f32 v83, v83, v147\n"
  "v_mfma_f32_16x16x32_bf16 v[120:123], a[56:59], v[76:79], v[120:123]\n"
  "v_cvt_pk_bf16_f32 v146, v80, v81\n"
  "v_cvt_pk_bf16_f32 v147, v82, v83\n"
  "v_mfma_f32_16x16x32_bf16 v[124:127], a[60:63], v[76:79], v[124:127]\n"
  "global_store_dwordx2 v58, v[146:147], s[56:57]\n"
  "v_add_u32 v58, 888832, v58\n"
  "buffer_load_dwordx4 v[76:79], v56, s[24:27], s67 offen sc1\n"
  "s_waitcnt vmcnt(14)\n"
  "v_mfma_f32_16x16x32_bf16 v[112:115], a[64:67], v[96:99], v[112:115]\n"
  "v_max3_u32 v7, v96, v97, v7\n"
  "v_mfma_f32_16x16x32_bf16 v[116:119], a[68:71], v[96:99], v[116:119]\n"
  "v_max3_u32 v7, v98, v99, v7\n"
  "v_mfma_f32_16x16x32_bf16 v[120:123], a[72:75], v[96:99], v[120:123]\n"
  "v_mfma_f32_16x16x32_bf16 v[124:127], a[76:79], v[96:99], v[124:127]\n"
  "buffer_load_dwordx4 v[96:99], v56, s[24:27], s68 offen sc1\n"
  "s_waitcnt vmcnt(14)\n"
  "v_mfma_f32_16x16x32_bf16 v[112:115], a[80:83], v[100:103], v[112:115]\n"
  "v_max3_u32 v7, v100, v101, v7\n"
  "v_mfma_f32_16x16x32_bf16 v[116:119], a[84:87], v[100:103], v[116:119]\n"
  "v_max3_u32 v7, v102, v103, v7\n"
  "v_mfma_f32_16x16x32_bf16 v[120:123], a[88:91], v[100:103], v[120:123]\n"
  "v_mfma_f32_16x16x32_bf16 v[124:127], a[92:95], v[100:103], v[124:127]\n"
  "buffer_load_dwordx4 v[100:103], v56, s[24:27], s69 offen sc1\n"
  "s_waitcnt vmcnt(14)\n"
  "v_mfma_f32_16x16x32_bf16 v[112:115], a[96:99], v[104:107], v[112:115]\n"
  "v_max3_u32 v7, v104, v105, v7\n"
  "v_mfma_f32_16x16x32_bf16 v[116:119], a[100:103], v[104:107], v[116:119]\n"
  "v_max3_u32 v7, v106, v107, v7\n"
  "v_mfma_f32_16x16x32_bf16 v[120:123], a[104:107], v[104:107], v[120:123]\n"
  "v_mfma_f32_16x16x32_bf16 v[124:127], a[108:111], v[104:107], v[124:127]\n"
  "buffer_load_dwordx4 v[104:107], v56, s[24:27], s70 offen sc1\n"
  "s_waitcnt vmcnt(14)\n"
  "v_mfma_f32_16x16x32_bf16 v[112:115], a[112:115], v[108:111], v[112:115]\n"
  "v_max3_u32 v7, v108, v109, v7\n"
  "v_mfma_f32_16x16x32_bf16 v[116:119], a[116:119], v[108:111], v[116:119]\n"
  "v_max3_u32 v7, v110, v111, v7\n"
  "v_mfma_f32_16x16x32_bf16 v[120:123], a[120:123], v[108:111], v[120:123]\n"
  "v_mfma_f32_16x16x32_bf16 v[124:127], a[124:127], v[108:111], v[124:127]\n"
  "buffer_load_dwordx4 v[108:111], v56, s[24:27], s71 offen sc1\n"
  "v_cmp_eq_u32 vcc, -1, v7\n"
  "s_nop 0\n"
  "s_and_b64 vcc, exec, vcc\n"
  "s_cbranch_vccnz L40_%=\n"
  "L41_%=:\n"
  "s_waitcnt vmcnt(14)\n"
  "v_mfma_f32_16x16x32_bf16 v[128:131], a[224:227], v[14:17], 0\n"
  "v_max3_u32 v7, v14, v15, 0\n"
  "v_mfma_f32_16x16x32_bf16 v[132:135], a[228:231], v[14:17], 0\n"
  "v_max3_u32 v7, v16, v17, v7\n"
  "v_mfma_f32_16x16x32_bf16 v[136:139], a[232:235], v[14:17], 0\n"
  "v_mfma_f32_16x16x32_bf16 v[140:143], a[236:239], v[14:17], 0\n"
  "s_waitcnt vmcnt(13)\n"
  "v_mfma_f32_16x16x32_bf16 v[128:131], a[240:243], v[216:219], v[128:131]\n"
  "v_max3_u32 v7, v216, v217, v7\n"
  "v_mfma_f32_16x16x32_bf16 v[132:135], a[244:247], v[216:219], v[132:135]\n"
  "v_max3_u32 v7, v218, v219, v7\n"
  "v_mfma_f32_16x16x32_bf16 v[136:139], a[248:251], v[216:219], v[136:139]\n"
  "v_mfma_f32_16x16x32_bf16 v[140:143], a[252:255], v[216:219], v[140:143]\n"
  "ds_write_b128 v59, v[116:119] offset:1024\n"
  "ds_write_b128 v60, v[120:123] offset:2048\n"
  "ds_write_b128 v61, v[124:127] offset:3072\n"
  "s_waitcnt vmcnt(12)\n"
  "v_mfma_f32_16x16x32_bf16 v[128:131], v[152:155], v[220:223], v[128:131]\n"
  "v_max3_u32 v7, v220, v221, v7\n"
  "v_mfma_f32_16x16x32_bf16 v[132:135], v[156:159], v[220:223], v[132:135]\n"
  "v_max3_u32 v7, v222, v223, v7\n"
  "v_mfma_f32_16x16x32_bf16 v[136:139], v[160:163], v[220:223], v[136:139]\n"
  "v_mfma_f32_16x16x32_bf16 v[140:143], v[164:167], v[220:223], v[140:143]\n"
  "s_waitcnt vmcnt(11)\n"
  "v_mfma_f32_16x16x32_bf16 v[128:131], v[168:171], v[224:227], v[128:131]\n"
  "v_max3_u32 v7, v224, v225, v7\n"
  "v_mfma_f32_16x16x32_bf16 v[132:135], v[172:175], v[224:227], v[132:135]\n"
  "v_max3_u32 v7, v226, v227, v7\n"
  "v_mfma_f32_16x16x32_bf16 v[136:139], v[176:179], v[224:227], v[136:139]\n"
  "v_mfma_f32_16x16x32_bf16 v[140:143], v[180:183], v[224:227], v[140:143]\n"
  "s_waitcnt lgkmcnt(0)\n"
  "s_barrier\n"
  "ds_read_b128 v[228:231], v62 offset:1024\n"
  "ds_read_b128 v[232:235], v62 offset:2048\n"
  "ds_read_b128 v[236:239], v62 offset:3072\n"
  "s_waitcnt vmcnt(10)\n"
  "v_mfma_f32_16x16x32_bf16 v[128:131], v[184:187], v[38:41], v[128:131]\n"
  "v_max3_u32 v7, v38, v39, v7\n"
  "v_mfma_f32_16x16x32_bf16 v[132:135], v[188:191], v[38:41], v[132:135]\n"
  "v_max3_u32 v7, v40, v41, v7\n"
  "v_mfma_f32_16x16x32_bf16 v[136:139], v[192:195], v[38:41], v[136:139]\n"
  "v_mfma_f32_16x16x32_bf16 v[140:143], v[196:199], v[38:41], v[140:143]\n"
  "s_waitcnt vmcnt(9)\n"
  "v_mfma_f32_16x16x32_bf16 v[128:131], v[200:203], v[42:45], v[128:131]\n"
  "v_max3_u32 v7, v42, v43, v7\n"
  "v_mfma_f32_16x16x32_bf16 v[132:135], v[204:207], v[42:45], v[132:135]\n"
  "v_max3_u32 v7, v44, v45, v7\n"
  "v_mfma_f32_16x16x32_bf16 v[136:139], v[208:211], v[42:45], v[136:139]\n"
  "v_mfma_f32_16x16x32_bf16 v[140:143], v[212:215], v[42:45], v[140:143]\n"
  "s_waitcnt vmcnt(36) lgkmcnt(0)\n"
  "s_waitcnt vmcnt(8)\n"
  "v_mfma_f32_16x16x32_bf16 v[128:131], a[0:3], v[64:67], v[128:131]\n"
  "v_max3_u32 v7, v64, v65, v7\n"
  "v_add_f32 v112, v112, v228\n"
  "v_mfma_f32_16x16x32_bf16 v[132:135], a[4:7], v[64:67], v[132:135]\n"
  "v_max3_u32 v7, v66, v67, v7\n"
  "v_add_f32 v113, v113, v229\n"
  "v_mfma_f32_16x16x32_bf16 v[136:139], a[8:11], v[64:67], v[136:139]\n"
  "v_add_f32 v114, v114, v230\n"
  "v_add_f32 v115, v115, v231\n"
  "v_mfma_f32_16x16x32_bf16 v[140:143], a[12:15], v[64:67], v[140:143]\n"
  "v_add_f32 v112, v112, v232\n"
  "v_add_f32 v113, v113, v233\n"
  "s_waitcnt vmcnt(7)\n"
  "v_mfma_f32_16x16x32_bf16 v[128:131], a[16:19], v[68:71], v[128:131]\n"
  "v_max3_u32 v7, v68, v69, v7\n"
  "v_add_f32 v114, v114, v234\n"
  "v_mfma_f32_16x16x32_bf16 v[132:135], a[20:23], v[68:71], v[132:135]\n"
  "v_max3_u32 v7, v70, v71, v7\n"
  "v_add_f32 v115, v115, v235\n"
  "v_mfma_f32_16x16x32_bf16 v[136:139], a[24:27], v[68:71], v[136:139]\n"
  "v_add_f32 v112, v112, v236\n"
  "v_add_f32 v113, v113, v237\n"
  "v_mfma_f32_16x16x32_bf16 v[140:143], a[28:31], v[68:71], v[140:143]\n"
  "v_add_f32 v114, v114, v238\n"
  "v_add_f32 v115, v115, v239\n"
  "s_waitcnt vmcnt(6)\n"
  "v_mfma_f32_16x16x32_bf16 v[128:131], a[32:35], v[72:75], v[128:131]\n"
  "v_max3_u32 v7, v72, v73, v7\n"
  "v_lshlrev_b32 v10, 16, v148\n"
  "v_mfma_f32_16x16x32_bf16 v[132:135], a[36:39], v[72:75], v[132:135]\n"
  "v_max3_u32 v7, v74, v75, v7\n"
  "v_and_b32 v148, 0xffff0000, v148\n"
  "v_mfma_f32_16x16x32_bf16 v[136:139], a[40:43], v[72:75], v[136:139]\n"
  "v_add_f32 v112, v112, v10\n"
  "v_add_f32 v113, v113, v148\n"
  "v_mfma_f32_16x16x32_bf16 v[140:143], a[44:47], v[72:75], v[140:143]\n"
  "v_lshlrev_b32 v10, 16, v149\n"
  "v_and_b32 v149, 0xffff0000, v149\n"
  "s_waitcnt vmcnt(4)\n"
  "v_mfma_f32_16x16x32_bf16 v[128:131], a[48:51], v[76:79], v[128:131]\n"
  "v_max3_u32 v7, v76, v77, v7\n"
  "v_add_f32 v114, v114, v10\n"
  "v_mfma_f32_16x16x32_bf16 v[132:135], a[52:55], v[76:79], v[132:135]\n"
  "v_max3_u32 v7, v78, v79, v7\n"
  "v_add_f32 v115, v115, v149\n"
  "v_mfma_f32_16x16x32_bf16 v[136:139], a[56:59], v[76:79], v[136:139]\n"
  "v_cvt_pk_bf16_f32 v148, v112, v113\n"
  "v_cvt_pk_bf16_f32 v149, v114, v115\n"
  "v_mfma_f32_16x16x32_bf16 v[140:143], a[60:63], v[76:79], v[140:143]\n"
  "global_store_dwordx2 v58, v[148:149], s[56:57]\n"
  "v_add_u32 v58, 28672, v58\n"
  "s_waitcnt vmcnt(4)\n"
  "v_mfma_f32_16x16x32_bf16 v[128:131], a[64:67], v[96:99], v[128:131]\n"
  "v_max3_u32 v7, v96, v97, v7\n"
  "v_mfma_f32_16x16x32_bf16 v[132:135], a[68:71], v[96:99], v[132:135]\n"
  "v_max3_u32 v7, v98, v99, v7\n"
  "v_mfma_f32_16x16x32_bf16 v[136:139], a[72:75], v[96:99], v[136:139]\n"
  "v_mfma_f32_16x16x32_bf16 v[140:143], a[76:79], v[96:99], v[140:143]\n"
  "s_waitcnt vmcnt(3)\n"
  "v_mfma_f32_16x16x32_bf16 v[128:131], a[80:83], v[100:103], v[128:131]\n"
  "v_max3_u32 v7, v100, v101, v7\n"
  "v_mfma_f32_16x16x32_bf16 v[132:135], a[84:87], v[100:103], v[132:135]\n"
  "v_max3_u32 v7, v102, v103, v7\n"
  "v_mfma_f32_16x16x32_bf16 v[136:139], a[88:91], v[100:103], v[136:139]\n"
  "v_mfma_f32_16x16x32_bf16 v[140:143], a[92:95], v[100:103], v[140:143]\n"
  "s_waitcnt vmcnt(2)\n"
  "v_mfma_f32_16x16x32_bf16 v[128:131], a[96:99], v[104:107], v[128:131]\n"
  "v_max3_u32 v7, v104, v105, v7\n"
  "v_mfma_f32_16x16x32_bf16 v[132:135], a[100:103], v[104:107], v[132:135]\n"
  "v_max3_u32 v7, v106, v107, v7\n"
  "v_mfma_f32_16x16x32_bf16 v[136:139], a[104:107], v[104:107], v[136:139]\n"
  "v_mfma_f32_16x16x32_bf16 v[140:143], a[108:111], v[104:107], v[140:143]\n"
  "s_waitcnt vmcnt(1)\n"
  "v_mfma_f32_16x16x32_bf16 v[128:131], a[112:115], v[108:111], v[128:131]\n"
  "v_max3_u32 v7, v108, v109, v7\n"
  "v_mfma_f32_16x16x32_bf16 v[132:135], a[116:119], v[108:111], v[132:135]\n"
  "v_max3_u32 v7, v110, v111, v7\n"
  "v_mfma_f32_16x16x32_bf16 v[136:139], a[120:123], v[108:111], v[136:139]\n"
  "v_mfma_f32_16x16x32_bf16 v[140:143], a[124:127], v[108:111], v[140:143]\n"
  "v_cmp_eq_u32 vcc, -1, v7\n"
  "s_nop 0\n"
  "s_and_b64 vcc, exec, vcc\n"
  "s_cbranch_vccnz L44_%=\n"
  "L45_%=:\n"
  "s_nop 0\n"
  "ds_write_b128 v59, v[132:135] offset:17408\n"
  "s_nop 0\n"
  "ds_write_b128 v60, v[136:139] offset:18432\n"
  "ds_write_b128 v61, v[140:143] offset:19456\n"
  "s_waitcnt lgkmcnt(0)\n"
  "s_barrier\n"
  "ds_read_b128 v[228:231], v62 offset:17408\n"
  "ds_read_b128 v[232:235], v62 offset:18432\n"
  "ds_read_b128 v[236:239], v62 offset:19456\n"
  "s_waitcnt lgkmcnt(0)\n"
  "v_add_f32 v128, v128, v228\n"
  "v_add_f32 v129, v129, v229\n"
  "v_add_f32 v130, v130, v230\n"
  "v_add_f32 v131, v131, v231\n"
  "v_add_f32 v128, v128, v232\n"
  "v_add_f32 v129, v129, v233\n"
  "v_add_f32 v130, v130, v234\n"
  "v_add_f32 v131, v131, v235\n"
  "v_add_f32 v128, v128, v236\n"
  "v_add_f32 v129, v129, v237\n"
  "v_add_f32 v130, v130, v238\n"
  "v_add_f32 v131, v131, v239\n"
  "v_lshlrev_b32 v10, 16, v150\n"
  "v_and_b32 v150, 0xffff0000, v150\n"
  "v_add_f32 v128, v128, v10\n"
  "v_add_f32 v129, v129, v150\n"
  "v_lshlrev_b32 v10, 16, v151\n"
  "v_and_b32 v151, 0xffff0000, v151\n"
  "v_add_f32 v130, v130, v10\n"
  "v_add_f32 v131, v131, v151\n"
  "v_cvt_pk_bf16_f32 v150, v128, v129\n"
  "v_cvt_pk_bf16_f32 v151, v130, v131\n"
  "global_store_dwordx2 v58, v[150:151], s[56:57]\n"
  "s_endpgm\n"
  "L1_%=:\n"
  "v_lshlrev_b32 v10, 8, v2\n"
  "v_mov_b32 v11, 0\n"
  "s_mov_b32 s5, 4096\n"
  "buffer_store_dword v11, v10, s[24:27], s5 offen sc1\n"
  "buffer_store_dword v11, v10, s[24:27], s5 offen offset:128 sc1\n"
  "s_waitcnt vmcnt(0)\n"
  "s_branch L2_%=\n"
  "L3_%=:\n"
  "v_lshlrev_b32 v52, 3, v8\n"
  "s_lshl_b32 s4, s98, 6\n"
  "s_add_u32 s4, s4, 131072\n"
  "v_lshl_add_u32 v22, v8, 4, s4\n"
  "s_waitcnt lgkmcnt(0)\n"
  "s_barrier\n"
  "ds_read_b64 v[20:21], v52 offset:16048\n"
  "s_waitcnt lgkmcnt(0)\n"
  "v_mov_b32 v50, v20\n"
  "v_mov_b32 v52, v21\n"
  "s_and_b64 exec, s[44:45], 15\n"
  "buffer_store_dwordx4 v[50:53], v22, s[24:27], 0 offen sc1\n"
  "s_mov_b64 exec, -1\n"
  "s_sub_u32 s43, s98, 224\n"
  "s_cmp_ge_u32 s43, 16\n"
  "s_cselect_b32 s64, s32, s28\n"
  "s_cselect_b32 s65, s33, s29\n"
  "s_cselect_b32 s66, s34, s30\n"
  "s_cselect_b32 s67, s35, s31\n"
  "s_and_b32 s4, s43, 15\n"
  "s_mul_i32 s4, s4, 224\n"
  "s_mul_i32 s5, s3, 401408\n"
  "s_add_u32 s4, s4, s5\n"
  "v_mul_u32_u24 v10, 4682, v2\n"
  "v_lshrrev_b32 v10, 16, v10\n"
  "v_mul_u32_u24 v6, 14, v10\n"
  "v_sub_u32 v6, v2, v6\n"
  "v_mul_u32_u24 v5, 3584, v10\n"
  "v_lshl_add_u32 v5, v6, 4, v5\n"
  "v_add_u32 v5, s4, v5\n"
  "v_cmp_gt_u32 vcc, 56, v2\n"
  "v_mov_b32 v11, 2146435072\n"
  "s_nop 0\n"
  "v_cndmask_b32 v5, v11, v5, vcc\n"
  "s_mov_b32 s19, 0\n"
  "buffer_load_dwordx4 v[32:35], v5, s[64:67], s19 offen\n"
  "s_add_u32 s19, s19, 14336\n"
  "buffer_load_dwordx4 v[54:57], v5, s[64:67], s19 offen\n"
  "s_add_u32 s19, s19, 14336\n"
  "buffer_load_dwordx4 v[58:61], v5, s[64:67], s19 offen\n"
  "s_add_u32 s19, s19, 14336\n"
  "buffer_load_dwordx4 v[62:65], v5, s[64:67], s19 offen\n"
  "s_add_u32 s19, s19, 14336\n"
  "buffer_load_dwordx4 v[66:69], v5, s[64:67], s19 offen\n"
  "s_add_u32 s19, s19, 14336\n"
  "buffer_load_dwordx4 v[70:73], v5, s[64:67], s19 offen\n"
  "s_add_u32 s19, s19, 14336\n"
  "buffer_load_dwordx4 v[74:77], v5, s[64:67], s19 offen\n"
  "s_add_u32 s19, s19, 14336\n"
  "buffer_load_dwordx4 v[78:81], v5, s[64:67], s19 offen\n"
  "s_add_u32 s19, s19, 14336\n"
  "buffer_load_dwordx4 v[82:85], v5, s[64:67], s19 offen\n"
  "s_add_u32 s19, s19, 14336\n"
  "buffer_load_dwordx4 v[86:89], v5, s[64:67], s19 offen\n"
  "s_add_u32 s19, s19, 14336\n"
  "buffer_load_dwordx4 v[90:93], v5, s[64:67], s19 offen\n"
  "s_add_u32 s19, s19, 14336\n"
  "buffer_load_dwordx4 v[94:97], v5, s[64:67], s19 offen\n"
  "s_add_u32 s19, s19, 14336\n"
  "buffer_load_dwordx4 v[98:101], v5, s[64:67], s19 offen\n"
  "s_add_u32 s19, s19, 14336\n"
  "buffer_load_dwordx4 v[102:105], v5, s[64:67], s19 offen\n"
  "s_add_u32 s19, s19, 14336\n"
  "buffer_load_dwordx4 v[106:109], v5, s[64:67], s19 offen\n"
  "s_add_u32 s19, s19, 14336\n"
  "buffer_load_dwordx4 v[110:113], v5, s[64:67], s19 offen\n"
  "s_add_u32 s19, s19, 14336\n"
  "buffer_load_dwordx4 v[114:117], v5, s[64:67], s19 offen\n"
  "s_add_u32 s19, s19, 14336\n"
  "buffer_load_dwordx4 v[118:121], v5, s[64:67], s19 offen\n"
  "s_add_u32 s19, s19, 14336\n"
  "buffer_load_dwordx4 v[122:125], v5, s[64:67], s19 offen\n"
  "s_add_u32 s19, s19, 14336\n"
  "buffer_load_dwordx4 v[126:129], v5, s[64:67], s19 offen\n"
  "s_add_u32 s19, s19, 14336\n"
  "buffer_load_dwordx4 v[130:133], v5, s[64:67], s19 offen\n"
  "s_add_u32 s19, s19, 14336\n"
  "buffer_load_dwordx4 v[134:137], v5, s[64:67], s19 offen\n"
  "s_add_u32 s19, s19, 14336\n"
  "buffer_load_dwordx4 v[138:141], v5, s[64:67], s19 offen\n"
  "s_add_u32 s19, s19, 14336\n"
  "buffer_load_dwordx4 v[142:145], v5, s[64:67], s19 offen\n"
  "s_add_u32 s19, s19, 14336\n"
  "buffer_load_dwordx4 v[146:149], v5, s[64:67], s19 offen\n"
  "s_add_u32 s19, s19, 14336\n"
  "buffer_load_dwordx4 v[150:153], v5, s[64:67], s19 offen\n"
  "s_add_u32 s19, s19, 14336\n"
  "buffer_load_dwordx4 v[154:157], v5, s[64:67], s19 offen\n"
  "s_add_u32 s19, s19, 14336\n"
  "buffer_load_dwordx4 v[158:161], v5, s[64:67], s19 offen\n"
  "s_add_u32 s19, s19, 14336\n"
  "s_mov_b32 s4, 0x00003f80\n"
  "s_mov_b32 s5, 0x3f800000\n"
  "s_waitcnt vmcnt(27)\n"
  "v_dot2_f32_bf16 v24, v32, s4, 0\n"
  "v_dot2_f32_bf16 v25, v32, s5, 0\n"
  "v_dot2_f32_bf16 v26, v33, s4, 0\n"
  "v_dot2_f32_bf16 v27, v33, s5, 0\n"
  "v_dot2_f32_bf16 v28, v34, s4, 0\n"
  "v_dot2_f32_bf16 v29, v34, s5, 0\n"
  "v_dot2_f32_bf16 v30, v35, s4, 0\n"
  "v_dot2_f32_bf16 v31, v35, s5, 0\n"
  "s_waitcnt vmcnt(26)\n"
  "v_dot2_f32_bf16 v24, v54, s4, v24\n"
  "v_dot2_f32_bf16 v25, v54, s5, v25\n"
  "v_dot2_f32_bf16 v26, v55, s4, v26\n"
  "v_dot2_f32_bf16 v27, v55, s5, v27\n"
  "v_dot2_f32_bf16 v28, v56, s4, v28\n"
  "v_dot2_f32_bf16 v29, v56, s5, v29\n"
  "v_dot2_f32_bf16 v30, v57, s4, v30\n"
  "v_dot2_f32_bf16 v31, v57, s5, v31\n"
  "s_waitcnt vmcnt(25)\n"
  "v_dot2_f32_bf16 v24, v58, s4, v24\n"
  "v_dot2_f32_bf16 v25, v58, s5, v25\n"
  "v_dot2_f32_bf16 v26, v59, s4, v26\n"
  "v_dot2_f32_bf16 v27, v59, s5, v27\n"
  "v_dot2_f32_bf16 v28, v60, s4, v28\n"
  "v_dot2_f32_bf16 v29, v60, s5, v29\n"
  "v_dot2_f32_bf16 v30, v61, s4, v30\n"
  "v_dot2_f32_bf16 v31, v61, s5, v31\n"
  "s_waitcnt vmcnt(24)\n"
  "v_dot2_f32_bf16 v24, v62, s4, v24\n"
  "v_dot2_f32_bf16 v25, v62, s5, v25\n"
  "v_dot2_f32_bf16 v26, v63, s4, v26\n"
  "v_dot2_f32_bf16 v27, v63, s5, v27\n"
  "v_dot2_f32_bf16 v28, v64, s4, v28\n"
  "v_dot2_f32_bf16 v29, v64, s5, v29\n"
  "v_dot2_f32_bf16 v30, v65, s4, v30\n"
  "v_dot2_f32_bf16 v31, v65, s5, v31\n"
  "s_waitcnt vmcnt(23)\n"
  "v_dot2_f32_bf16 v24, v66, s4, v24\n"
  "v_dot2_f32_bf16 v25, v66, s5, v25\n"
  "v_dot2_f32_bf16 v26, v67, s4, v26\n"
  "v_dot2_f32_bf16 v27, v67, s5, v27\n"
  "v_dot2_f32_bf16 v28, v68, s4, v28\n"
  "v_dot2_f32_bf16 v29, v68, s5, v29\n"
  "v_dot2_f32_bf16 v30, v69, s4, v30\n"
  "v_dot2_f32_bf16 v31, v69, s5, v31\n"
  "s_waitcnt vmcnt(22)\n"
  "v_dot2_f32_bf16 v24, v70, s4, v24\n"
  "v_dot2_f32_bf16 v25, v70, s5, v25\n"
  "v_dot2_f32_bf16 v26, v71, s4, v26\n"
  "v_dot2_f32_bf16 v27, v71, s5, v27\n"
  "v_dot2_f32_bf16 v28, v72, s4, v28\n"
  "v_dot2_f32_bf16 v29, v72, s5, v29\n"
  "v_dot2_f32_bf16 v30, v73, s4, v30\n"
  "v_dot2_f32_bf16 v31, v73, s5, v31\n"
  "s_waitcnt vmcnt(21)\n"
  "v_dot2_f32_bf16 v24, v74, s4, v24\n"
  "v_dot2_f32_bf16 v25, v74, s5, v25\n"
  "v_dot2_f32_bf16 v26, v75, s4, v26\n"
  "v_dot2_f32_bf16 v27, v75, s5, v27\n"
  "v_dot2_f32_bf16 v28, v76, s4, v28\n"
  "v_dot2_f32_bf16 v29, v76, s5, v29\n"
  "v_dot2_f32_bf16 v30, v77, s4, v30\n"
  "v_dot2_f32_bf16 v31, v77, s5, v31\n"
  "s_waitcnt vmcnt(20)\n"
  "v_dot2_f32_bf16 v24, v78, s4, v24\n"
  "v_dot2_f32_bf16 v25, v78, s5, v25\n"
  "v_dot2_f32_bf16 v26, v79, s4, v26\n"
  "v_dot2_f32_bf16 v27, v79, s5, v27\n"
  "v_dot2_f32_bf16 v28, v80, s4, v28\n"
  "v_dot2_f32_bf16 v29, v80, s5, v29\n"
  "v_dot2_f32_bf16 v30, v81, s4, v30\n"
  "v_dot2_f32_bf16 v31, v81, s5, v31\n"
  "s_waitcnt vmcnt(19)\n"
  "v_dot2_f32_bf16 v24, v82, s4, v24\n"
  "v_dot2_f32_bf16 v25, v82, s5, v25\n"
  "v_dot2_f32_bf16 v26, v83, s4, v26\n"
  "v_dot2_f32_bf16 v27, v83, s5, v27\n"
  "v_dot2_f32_bf16 v28, v84, s4, v28\n"
  "v_dot2_f32_bf16 v29, v84, s5, v29\n"
  "v_dot2_f32_bf16 v30, v85, s4, v30\n"
  "v_dot2_f32_bf16 v31, v85, s5, v31\n"
  "s_waitcnt vmcnt(18)\n"
  "v_dot2_f32_bf16 v24, v86, s4, v24\n"
  "v_dot2_f32_bf16 v25, v86, s5, v25\n"
  "v_dot2_f32_bf16 v26, v87, s4, v26\n"
  "v_dot2_f32_bf16 v27, v87, s5, v27\n"
  "v_dot2_f32_bf16 v28, v88, s4, v28\n"
  "v_dot2_f32_bf16 v29, v88, s5, v29\n"
  "v_dot2_f32_bf16 v30, v89, s4, v30\n"
  "v_dot2_f32_bf16 v31, v89, s5, v31\n"
  "s_waitcnt vmcnt(17)\n"
  "v_dot2_f32_bf16 v24, v90, s4, v24\n"
  "v_dot2_f32_bf16 v25, v90, s5, v25\n"
  "v_dot2_f32_bf16 v26, v91, s4, v26\n"
  "v_dot2_f32_bf16 v27, v91, s5, v27\n"
  "v_dot2_f32_bf16 v28, v92, s4, v28\n"
  "v_dot2_f32_bf16 v29, v92, s5, v29\n"
  "v_dot2_f32_bf16 v30, v93, s4, v30\n"
  "v_dot2_f32_bf16 v31, v93, s5, v31\n"
  "s_waitcnt vmcnt(16)\n"
  "v_dot2_f32_bf16 v24, v94, s4, v24\n"
  "v_dot2_f32_bf16 v25, v94, s5, v25\n"
  "v_dot2_f32_bf16 v26, v95, s4, v26\n"
  "v_dot2_f32_bf16 v27, v95, s5, v27\n"
  "v_dot2_f32_bf16 v28, v96, s4, v28\n"
  "v_dot2_f32_bf16 v29, v96, s5, v29\n"
  "v_dot2_f32_bf16 v30, v97, s4, v30\n"
  "v_dot2_f32_bf16 v31, v97, s5, v31\n"
  "s_waitcnt vmcnt(15)\n"
  "v_dot2_f32_bf16 v24, v98, s4, v24\n"
  "v_dot2_f32_bf16 v25, v98, s5, v25\n"
  "v_dot2_f32_bf16 v26, v99, s4, v26\n"
  "v_dot2_f32_bf16 v27, v99, s5, v27\n"
  "v_dot2_f32_bf16 v28, v100, s4, v28\n"
  "v_dot2_f32_bf16 v29, v100, s5, v29\n"
  "v_dot2_f32_bf16 v30, v101, s4, v30\n"
  "v_dot2_f32_bf16 v31, v101, s5, v31\n"
  "s_waitcnt vmcnt(14)\n"
  "v_dot2_f32_bf16 v24, v102, s4, v24\n"
  "v_dot2_f32_bf16 v25, v102, s5, v25\n"
  "v_dot2_f32_bf16 v26, v103, s4, v26\n"
  "v_dot2_f32_bf16 v27, v103, s5, v27\n"
  "v_dot2_f32_bf16 v28, v104, s4, v28\n"
  "v_dot2_f32_bf16 v29, v104, s5, v29\n"
  "v_dot2_f32_bf16 v30, v105, s4, v30\n"
  "v_dot2_f32_bf16 v31, v105, s5, v31\n"
  "s_waitcnt vmcnt(13)\n"
  "v_dot2_f32_bf16 v24, v106, s4, v24\n"
  "v_dot2_f32_bf16 v25, v106, s5, v25\n"
  "v_dot2_f32_bf16 v26, v107, s4, v26\n"
  "v_dot2_f32_bf16 v27, v107, s5, v27\n"
  "v_dot2_f32_bf16 v28, v108, s4, v28\n"
  "v_dot2_f32_bf16 v29, v108, s5, v29\n"
  "v_dot2_f32_bf16 v30, v109, s4, v30\n"
  "v_dot2_f32_bf16 v31, v109, s5, v31\n"
  "s_waitcnt vmcnt(12)\n"
  "v_dot2_f32_bf16 v24, v110, s4, v24\n"
  "v_dot2_f32_bf16 v25, v110, s5, v25\n"
  "v_dot2_f32_bf16 v26, v111, s4, v26\n"
  "v_dot2_f32_bf16 v27, v111, s5, v27\n"
  "v_dot2_f32_bf16 v28, v112, s4, v28\n"
  "v_dot2_f32_bf16 v29, v112, s5, v29\n"
  "v_dot2_f32_bf16 v30, v113, s4, v30\n"
  "v_dot2_f32_bf16 v31, v113, s5, v31\n"
  "s_waitcnt vmcnt(11)\n"
  "v_dot2_f32_bf16 v24, v114, s4, v24\n"
  "v_dot2_f32_bf16 v25, v114, s5, v25\n"
  "v_dot2_f32_bf16 v26, v115, s4, v26\n"
  "v_dot2_f32_bf16 v27, v115, s5, v27\n"
  "v_dot2_f32_bf16 v28, v116, s4, v28\n"
  "v_dot2_f32_bf16 v29, v116, s5, v29\n"
  "v_dot2_f32_bf16 v30, v117, s4, v30\n"
  "v_dot2_f32_bf16 v31, v117, s5, v31\n"
  "s_waitcnt vmcnt(10)\n"
  "v_dot2_f32_bf16 v24, v118, s4, v24\n"
  "v_dot2_f32_bf16 v25, v118, s5, v25\n"
  "v_dot2_f32_bf16 v26, v119, s4, v26\n"
  "v_dot2_f32_bf16 v27, v119, s5, v27\n"
  "v_dot2_f32_bf16 v28, v120, s4, v28\n"
  "v_dot2_f32_bf16 v29, v120, s5, v29\n"
  "v_dot2_f32_bf16 v30, v121, s4, v30\n"
  "v_dot2_f32_bf16 v31, v121, s5, v31\n"
  "s_waitcnt vmcnt(9)\n"
  "v_dot2_f32_bf16 v24, v122, s4, v24\n"
  "v_dot2_f32_bf16 v25, v122, s5, v25\n"
  "v_dot2_f32_bf16 v26, v123, s4, v26\n"
  "v_dot2_f32_bf16 v27, v123, s5, v27\n"
  "v_dot2_f32_bf16 v28, v124, s4, v28\n"
  "v_dot2_f32_bf16 v29, v124, s5, v29\n"
  "v_dot2_f32_bf16 v30, v125, s4, v30\n"
  "v_dot2_f32_bf16 v31, v125, s5, v31\n"
  "s_waitcnt vmcnt(8)\n"
  "v_dot2_f32_bf16 v24, v126, s4, v24\n"
  "v_dot2_f32_bf16 v25, v126, s5, v25\n"
  "v_dot2_f32_bf16 v26, v127, s4, v26\n"
  "v_dot2_f32_bf16 v27, v127, s5, v27\n"
  "v_dot2_f32_bf16 v28, v128, s4, v28\n"
  "v_dot2_f32_bf16 v29, v128, s5, v29\n"
  "v_dot2_f32_bf16 v30, v129, s4, v30\n"
  "v_dot2_f32_bf16 v31, v129, s5, v31\n"
  "s_waitcnt vmcnt(7)\n"
  "v_dot2_f32_bf16 v24, v130, s4, v24\n"
  "v_dot2_f32_bf16 v25, v130, s5, v25\n"
  "v_dot2_f32_bf16 v26, v131, s4, v26\n"
  "v_dot2_f32_bf16 v27, v131, s5, v27\n"
  "v_dot2_f32_bf16 v28, v132, s4, v28\n"
  "v_dot2_f32_bf16 v29, v132, s5, v29\n"
  "v_dot2_f32_bf16 v30, v133, s4, v30\n"
  "v_dot2_f32_bf16 v31, v133, s5, v31\n"
  "s_waitcnt vmcnt(6)\n"
  "v_dot2_f32_bf16 v24, v134, s4, v24\n"
  "v_dot2_f32_bf16 v25, v134, s5, v25\n"
  "v_dot2_f32_bf16 v26, v135, s4, v26\n"
  "v_dot2_f32_bf16 v27, v135, s5, v27\n"
  "v_dot2_f32_bf16 v28, v136, s4, v28\n"
  "v_dot2_f32_bf16 v29, v136, s5, v29\n"
  "v_dot2_f32_bf16 v30, v137, s4, v30\n"
  "v_dot2_f32_bf16 v31, v137, s5, v31\n"
  "s_waitcnt vmcnt(5)\n"
  "v_dot2_f32_bf16 v24, v138, s4, v24\n"
  "v_dot2_f32_bf16 v25, v138, s5, v25\n"
  "v_dot2_f32_bf16 v26, v139, s4, v26\n"
  "v_dot2_f32_bf16 v27, v139, s5, v27\n"
  "v_dot2_f32_bf16 v28, v140, s4, v28\n"
  "v_dot2_f32_bf16 v29, v140, s5, v29\n"
  "v_dot2_f32_bf16 v30, v141, s4, v30\n"
  "v_dot2_f32_bf16 v31, v141, s5, v31\n"
  "s_waitcnt vmcnt(4)\n"
  "v_dot2_f32_bf16 v24, v142, s4, v24\n"
  "v_dot2_f32_bf16 v25, v142, s5, v25\n"
  "v_dot2_f32_bf16 v26, v143, s4, v26\n"
  "v_dot2_f32_bf16 v27, v143, s5, v27\n"
  "v_dot2_f32_bf16 v28, v144, s4, v28\n"
  "v_dot2_f32_bf16 v29, v144, s5, v29\n"
  "v_dot2_f32_bf16 v30, v145, s4, v30\n"
  "v_dot2_f32_bf16 v31, v145, s5, v31\n"
  "s_waitcnt vmcnt(3)\n"
  "v_dot2_f32_bf16 v24, v146, s4, v24\n"
  "v_dot2_f32_bf16 v25, v146, s5, v25\n"
  "v_dot2_f32_bf16 v26, v147, s4, v26\n"
  "v_dot2_f32_bf16 v27, v147, s5, v27\n"
  "v_dot2_f32_bf16 v28, v148, s4, v28\n"
  "v_dot2_f32_bf16 v29, v148, s5, v29\n"
  "v_dot2_f32_bf16 v30, v149, s4, v30\n"
  "v_dot2_f32_bf16 v31, v149, s5, v31\n"
  "s_waitcnt vmcnt(2)\n"
  "v_dot2_f32_bf16 v24, v150, s4, v24\n"
  "v_dot2_f32_bf16 v25, v150, s5, v25\n"
  "v_dot2_f32_bf16 v26, v151, s4, v26\n"
  "v_dot2_f32_bf16 v27, v151, s5, v27\n"
  "v_dot2_f32_bf16 v28, v152, s4, v28\n"
  "v_dot2_f32_bf16 v29, v152, s5, v29\n"
  "v_dot2_f32_bf16 v30, v153, s4, v30\n"
  "v_dot2_f32_bf16 v31, v153, s5, v31\n"
  "s_waitcnt vmcnt(1)\n"
  "v_dot2_f32_bf16 v24, v154, s4, v24\n"
  "v_dot2_f32_bf16 v25, v154, s5, v25\n"
  "v_dot2_f32_bf16 v26, v155, s4, v26\n"
  "v_dot2_f32_bf16 v27, v155, s5, v27\n"
  "v_dot2_f32_bf16 v28, v156, s4, v28\n"
  "v_dot2_f32_bf16 v29, v156, s5, v29\n"
  "v_dot2_f32_bf16 v30, v157, s4, v30\n"
  "v_dot2_f32_bf16 v31, v157, s5, v31\n"
  "s_waitcnt vmcnt(0)\n"
  "v_dot2_f32_bf16 v24, v158, s4, v24\n"
  "v_dot2_f32_bf16 v25, v158, s5, v25\n"
  "v_dot2_f32_bf16 v26, v159, s4, v26\n"
  "v_dot2_f32_bf16 v27, v159, s5, v27\n"
  "v_dot2_f32_bf16 v28, v160, s4, v28\n"
  "v_dot2_f32_bf16 v29, v160, s5, v29\n"
  "v_dot2_f32_bf16 v30, v161, s4, v30\n"
  "v_dot2_f32_bf16 v31, v161, s5, v31\n"
  "s_lshl_b32 s4, s3, 2\n"
  "v_add_u32 v10, s4, v10\n"
  "v_mul_u32_u24 v10, 448, v10\n"
  "v_lshl_add_u32 v10, v6, 5, v10\n"
  "s_mov_b64 exec, vcc\n"
  "ds_write_b128 v10, v[24:27]\n"
  "ds_write_b128 v10, v[28:31] offset:16\n"
  "s_mov_b64 exec, -1\n"
  "s_waitcnt lgkmcnt(0)\n"
  "s_barrier\n"
  "v_lshlrev_b32 v10, 2, v1\n"
  "ds_read_b32 v32, v10 offset:0\n"
  "ds_read_b32 v54, v10 offset:448\n"
  "ds_read_b32 v58, v10 offset:896\n"
  "ds_read_b32 v62, v10 offset:1344\n"
  "ds_read_b32 v66, v10 offset:1792\n"
  "ds_read_b32 v70, v10 offset:2240\n"
  "ds_read_b32 v74, v10 offset:2688\n"
  "ds_read_b32 v78, v10 offset:3136\n"
  "ds_read_b32 v82, v10 offset:3584\n"
  "ds_read_b32 v86, v10 offset:4032\n"
  "ds_read_b32 v90, v10 offset:4480\n"
  "ds_read_b32 v94, v10 offset:4928\n"
  "ds_read_b32 v98, v10 offset:5376\n"
  "ds_read_b32 v102, v10 offset:5824\n"
  "ds_read_b32 v106, v10 offset:6272\n"
  "ds_read_b32 v110, v10 offset:6720\n"
  "s_waitcnt lgkmcnt(0)\n"
  "v_add_f32 v32, v32, v54\n"
  "v_add_f32 v32, v32, v58\n"
  "v_add_f32 v32, v32, v62\n"
  "v_add_f32 v32, v32, v66\n"
  "v_add_f32 v32, v32, v70\n"
  "v_add_f32 v32, v32, v74\n"
  "v_add_f32 v32, v32, v78\n"
  "v_add_f32 v32, v32, v82\n"
  "v_add_f32 v32, v32, v86\n"
  "v_add_f32 v32, v32, v90\n"
  "v_add_f32 v32, v32, v94\n"
  "v_add_f32 v32, v32, v98\n"
  "v_add_f32 v32, v32, v102\n"
  "v_add_f32 v32, v32, v106\n"
  "v_add_f32 v32, v32, v110\n"
  "v_mov_b32 v33, s8\n"
  "s_mul_i32 s4, s43, 896\n"
  "s_add_u32 s4, s4, 196608\n"
  "v_lshl_add_u32 v10, v1, 3, s4\n"
  "v_cmp_gt_u32 vcc, 112, v1\n"
  "s_nop 1\n"
  "v_cndmask_b32 v10, v11, v10, vcc\n"
  "buffer_store_dwordx2 v[32:33], v10, s[24:27], 0 offen sc1\n"
  "s_mul_i32 s4, s43, 50176\n"
  "v_lshl_add_u32 v10, v1, 7, s4\n"
  "v_add_u32 v11, 32768, v10\n"
  "v_min_u32 v11, 1605504, v11\n"
  "global_load_dword v54, v10, s[54:55]\n"
  "global_load_dword v55, v11, s[54:55]\n"
  "s_mov_b32 s19, 400000\n"
  "v_mov_b32 v10, 147396\n"
  "L4_%=:\n"
  "buffer_load_dword v11, v10, s[24:27], 0 offen sc1\n"
  "s_waitcnt vmcnt(0)\n"
  "v_cmp_ne_u32 vcc, s8, v11\n"
  "s_nop 0\n"
  "s_and_b64 vcc, exec, vcc\n"
  "s_cbranch_vccz L5_%=\n"
  "s_sleep 8\n"
  "s_sub_u32 s19, s19, 1\n"
  "s_cbranch_scc0 L4_%=\n"
  "L5_%=:\n"
  "s_mov_b32 s19, 400000\n"
  "s_and_b32 s4, s43, 1\n"
  "s_lshl_b32 s4, s4, 7\n"
  "v_add_u32 v10, s4, v45\n"
  "s_lshr_b32 s4, s43, 1\n"
  "s_lshl_b32 s4, s4, 6\n"
  "s_and_b32 s5, s43, 1\n"
  "s_lshl_b32 s5, s5, 10\n"
  "s_add_u32 s43, s4, s5\n"
  "s_lshl_b32 s4, s43, 2\n"
  "s_mul_i32 s5, s3, 458752\n"
  "s_add_u32 s4, s4, s5\n"
  "s_add_u32 s4, s4, 5242880\n"
  "v_lshrrev_b32 v5, 4, v2\n"
  "v_lshlrev_b32 v5, 13, v5\n"
  "v_and_b32 v6, 15, v2\n"
  "v_lshl_add_u32 v5, v6, 4, v5\n"
  "v_add_u32 v5, s4, v5\n"
  "s_cmp_lg_u32 s3, 0\n"
  "s_cbranch_scc1 L8_%=\n"
  "L6_%=:\n"
  "buffer_load_dword v11, v10, s[24:27], 0 offen sc1\n"
  "s_waitcnt vmcnt(0)\n"
  "v_cmp_gt_u32 vcc, 28, v11\n"
  "s_nop 0\n"
  "s_and_b64 vcc, exec, vcc\n"
  "s_cbranch_vccz L7_%=\n"
  "s_sleep 8\n"
  "s_sub_u32 s19, s19, 1\n"
  "s_cbranch_scc0 L6_%=\n"
  "L7_%=:\n"
  "L8_%=:\n"
  "s_waitcnt lgkmcnt(0)\n"
  "s_barrier\n"
  "s_mov_b32 s19, 0\n"
  "buffer_load_dwordx4 v[32:35], v5, s[24:27], s19 offen sc1\n"
  "s_add_u32 s19, s19, 32768\n"
  "buffer_load_dwordx4 v[54:57], v5, s[24:27], s19 offen sc1\n"
  "s_add_u32 s19, s19, 32768\n"
  "buffer_load_dwordx4 v[58:61], v5, s[24:27], s19 offen sc1\n"
  "s_add_u32 s19, s19, 32768\n"
  "buffer_load_dwordx4 v[62:65], v5, s[24:27], s19 offen sc1\n"
  "s_add_u32 s19, s19, 32768\n"
  "buffer_load_dwordx4 v[66:69], v5, s[24:27], s19 offen sc1\n"
  "s_add_u32 s19, s19, 32768\n"
  "buffer_load_dwordx4 v[70:73], v5, s[24:27], s19 offen sc1\n"
  "s_add_u32 s19, s19, 32768\n"
  "buffer_load_dwordx4 v[74:77], v5, s[24:27], s19 offen sc1\n"
  "s_add_u32 s19, s19, 32768\n"
  "buffer_load_dwordx4 v[78:81], v5, s[24:27], s19 offen sc1\n"
  "s_add_u32 s19, s19, 32768\n"
  "buffer_load_dwordx4 v[82:85], v5, s[24:27], s19 offen sc1\n"
  "s_add_u32 s19, s19, 32768\n"
  "buffer_load_dwordx4 v[86:89], v5, s[24:27], s19 offen sc1\n"
  "s_add_u32 s19, s19, 32768\n"
  "buffer_load_dwordx4 v[90:93], v5, s[24:27], s19 offen sc1\n"
  "s_add_u32 s19, s19, 32768\n"
  "buffer_load_dwordx4 v[94:97], v5, s[24:27], s19 offen sc1\n"
  "s_add_u32 s19, s19, 32768\n"
  "buffer_load_dwordx4 v[98:101], v5, s[24:27], s19 offen sc1\n"
  "s_add_u32 s19, s19, 32768\n"
  "buffer_load_dwordx4 v[102:105], v5, s[24:27], s19 offen sc1\n"
  "s_add_u32 s19, s19, 32768\n"
  "s_mov_b32 s4, 0x3f803f80\n"
  "s_waitcnt vmcnt(13)\n"
  "v_dot2_f32_bf16 v24, v32, s4, 0\n"
  "v_dot2_f32_bf16 v28, v32, v32, 0\n"
  "v_dot2_f32_bf16 v25, v33, s4, 0\n"
  "v_dot2_f32_bf16 v29, v33, v33, 0\n"
  "v_dot2_f32_bf16 v26, v34, s4, 0\n"
  "v_dot2_f32_bf16 v30, v34, v34, 0\n"
  "v_dot2_f32_bf16 v27, v35, s4, 0\n"
  "v_dot2_f32_bf16 v31, v35, v35, 0\n"
  "s_waitcnt vmcnt(12)\n"
  "v_dot2_f32_bf16 v24, v54, s4, v24\n"
  "v_dot2_f32_bf16 v28, v54, v54, v28\n"
  "v_dot2_f32_bf16 v25, v55, s4, v25\n"
  "v_dot2_f32_bf16 v29, v55, v55, v29\n"
  "v_dot2_f32_bf16 v26, v56, s4, v26\n"
  "v_dot2_f32_bf16 v30, v56, v56, v30\n"
  "v_dot2_f32_bf16 v27, v57, s4, v27\n"
  "v_dot2_f32_bf16 v31, v57, v57, v31\n"
  "s_waitcnt vmcnt(11)\n"
  "v_dot2_f32_bf16 v24, v58, s4, v24\n"
  "v_dot2_f32_bf16 v28, v58, v58, v28\n"
  "v_dot2_f32_bf16 v25, v59, s4, v25\n"
  "v_dot2_f32_bf16 v29, v59, v59, v29\n"
  "v_dot2_f32_bf16 v26, v60, s4, v26\n"
  "v_dot2_f32_bf16 v30, v60, v60, v30\n"
  "v_dot2_f32_bf16 v27, v61, s4, v27\n"
  "v_dot2_f32_bf16 v31, v61, v61, v31\n"
  "s_waitcnt vmcnt(10)\n"
  "v_dot2_f32_bf16 v24, v62, s4, v24\n"
  "v_dot2_f32_bf16 v28, v62, v62, v28\n"
  "v_dot2_f32_bf16 v25, v63, s4, v25\n"
  "v_dot2_f32_bf16 v29, v63, v63, v29\n"
  "v_dot2_f32_bf16 v26, v64, s4, v26\n"
  "v_dot2_f32_bf16 v30, v64, v64, v30\n"
  "v_dot2_f32_bf16 v27, v65, s4, v27\n"
  "v_dot2_f32_bf16 v31, v65, v65, v31\n"
  "s_waitcnt vmcnt(9)\n"
  "v_dot2_f32_bf16 v24, v66, s4, v24\n"
  "v_dot2_f32_bf16 v28, v66, v66, v28\n"
  "v_dot2_f32_bf16 v25, v67, s4, v25\n"
  "v_dot2_f32_bf16 v29, v67, v67, v29\n"
  "v_dot2_f32_bf16 v26, v68, s4, v26\n"
  "v_dot2_f32_bf16 v30, v68, v68, v30\n"
  "v_dot2_f32_bf16 v27, v69, s4, v27\n"
  "v_dot2_f32_bf16 v31, v69, v69, v31\n"
  "s_waitcnt vmcnt(8)\n"
  "v_dot2_f32_bf16 v24, v70, s4, v24\n"
  "v_dot2_f32_bf16 v28, v70, v70, v28\n"
  "v_dot2_f32_bf16 v25, v71, s4, v25\n"
  "v_dot2_f32_bf16 v29, v71, v71, v29\n"
  "v_dot2_f32_bf16 v26, v72, s4, v26\n"
  "v_dot2_f32_bf16 v30, v72, v72, v30\n"
  "v_dot2_f32_bf16 v27, v73, s4, v27\n"
  "v_dot2_f32_bf16 v31, v73, v73, v31\n"
  "s_waitcnt vmcnt(7)\n"
  "v_dot2_f32_bf16 v24, v74, s4, v24\n"
  "v_dot2_f32_bf16 v28, v74, v74, v28\n"
  "v_dot2_f32_bf16 v25, v75, s4, v25\n"
  "v_dot2_f32_bf16 v29, v75, v75, v29\n"
  "v_dot2_f32_bf16 v26, v76, s4, v26\n"
  "v_dot2_f32_bf16 v30, v76, v76, v30\n"
  "v_dot2_f32_bf16 v27, v77, s4, v27\n"
  "v_dot2_f32_bf16 v31, v77, v77, v31\n"
  "s_waitcnt vmcnt(6)\n"
  "v_dot2_f32_bf16 v24, v78, s4, v24\n"
  "v_dot2_f32_bf16 v28, v78, v78, v28\n"
  "v_dot2_f32_bf16 v25, v79, s4, v25\n"
  "v_dot2_f32_bf16 v29, v79, v79, v29\n"
  "v_dot2_f32_bf16 v26, v80, s4, v26\n"
  "v_dot2_f32_bf16 v30, v80, v80, v30\n"
  "v_dot2_f32_bf16 v27, v81, s4, v27\n"
  "v_dot2_f32_bf16 v31, v81, v81, v31\n"
  "s_waitcnt vmcnt(5)\n"
  "v_dot2_f32_bf16 v24, v82, s4, v24\n"
  "v_dot2_f32_bf16 v28, v82, v82, v28\n"
  "v_dot2_f32_bf16 v25, v83, s4, v25\n"
  "v_dot2_f32_bf16 v29, v83, v83, v29\n"
  "v_dot2_f32_bf16 v26, v84, s4, v26\n"
  "v_dot2_f32_bf16 v30, v84, v84, v30\n"
  "v_dot2_f32_bf16 v27, v85, s4, v27\n"
  "v_dot2_f32_bf16 v31, v85, v85, v31\n"
  "s_waitcnt vmcnt(4)\n"
  "v_dot2_f32_bf16 v24, v86, s4, v24\n"
  "v_dot2_f32_bf16 v28, v86, v86, v28\n"
  "v_dot2_f32_bf16 v25, v87, s4, v25\n"
  "v_dot2_f32_bf16 v29, v87, v87, v29\n"
  "v_dot2_f32_bf16 v26, v88, s4, v26\n"
  "v_dot2_f32_bf16 v30, v88, v88, v30\n"
  "v_dot2_f32_bf16 v27, v89, s4, v27\n"
  "v_dot2_f32_bf16 v31, v89, v89, v31\n"
  "s_waitcnt vmcnt(3)\n"
  "v_dot2_f32_bf16 v24, v90, s4, v24\n"
  "v_dot2_f32_bf16 v28, v90, v90, v28\n"
  "v_dot2_f32_bf16 v25, v91, s4, v25\n"
  "v_dot2_f32_bf16 v29, v91, v91, v29\n"
  "v_dot2_f32_bf16 v26, v92, s4, v26\n"
  "v_dot2_f32_bf16 v30, v92, v92, v30\n"
  "v_dot2_f32_bf16 v27, v93, s4, v27\n"
  "v_dot2_f32_bf16 v31, v93, v93, v31\n"
  "s_waitcnt vmcnt(2)\n"
  "v_dot2_f32_bf16 v24, v94, s4, v24\n"
  "v_dot2_f32_bf16 v28, v94, v94, v28\n"
  "v_dot2_f32_bf16 v25, v95, s4, v25\n"
  "v_dot2_f32_bf16 v29, v95, v95, v29\n"
  "v_dot2_f32_bf16 v26, v96, s4, v26\n"
  "v_dot2_f32_bf16 v30, v96, v96, v30\n"
  "v_dot2_f32_bf16 v27, v97, s4, v27\n"
  "v_dot2_f32_bf16 v31, v97, v97, v31\n"
  "s_waitcnt vmcnt(1)\n"
  "v_dot2_f32_bf16 v24, v98, s4, v24\n"
  "v_dot2_f32_bf16 v28, v98, v98, v28\n"
  "v_dot2_f32_bf16 v25, v99, s4, v25\n"
  "v_dot2_f32_bf16 v29, v99, v99, v29\n"
  "v_dot2_f32_bf16 v26, v100, s4, v26\n"
  "v_dot2_f32_bf16 v30, v100, v100, v30\n"
  "v_dot2_f32_bf16 v27, v101, s4, v27\n"
  "v_dot2_f32_bf16 v31, v101, v101, v31\n"
  "s_waitcnt vmcnt(0)\n"
  "v_dot2_f32_bf16 v24, v102, s4, v24\n"
  "v_dot2_f32_bf16 v28, v102, v102, v28\n"
  "v_dot2_f32_bf16 v25, v103, s4, v25\n"
  "v_dot2_f32_bf16 v29, v103, v103, v29\n"
  "v_dot2_f32_bf16 v26, v104, s4, v26\n"
  "v_dot2_f32_bf16 v30, v104, v104, v30\n"
  "v_dot2_f32_bf16 v27, v105, s4, v27\n"
  "v_dot2_f32_bf16 v31, v105, v105, v31\n"
  "v_lshrrev_b32 v10, 4, v1\n"
  "v_lshlrev_b32 v10, 9, v10\n"
  "v_lshl_add_u32 v10, v6, 4, v10\n"
  "ds_write_b128 v10, v[24:27] offset:8192\n"
  "ds_write_b128 v10, v[28:31] offset:8448\n"
  "s_waitcnt lgkmcnt(0)\n"
  "s_barrier\n"
  "s_cmp_lg_u32 s3, 0\n"
  "s_cbranch_scc1 L9_%=\n"
  "v_lshlrev_b32 v10, 2, v2\n"
  "ds_read2st64_b32 v[32:33], v10 offset0:32 offset1:33\n"
  "ds_read2st64_b32 v[54:55], v10 offset0:34 offset1:35\n"
  "ds_read2st64_b32 v[58:59], v10 offset0:36 offset1:37\n"
  "ds_read2st64_b32 v[62:63], v10 offset0:38 offset1:39\n"
  "ds_read2st64_b32 v[66:67], v10 offset0:40 offset1:41\n"
  "ds_read2st64_b32 v[70:71], v10 offset0:42 offset1:43\n"
  "ds_read2st64_b32 v[74:75], v10 offset0:44 offset1:45\n"
  "ds_read2st64_b32 v[78:79], v10 offset0:46 offset1:47\n"
  "ds_read2st64_b32 v[82:83], v10 offset0:48 offset1:49\n"
  "ds_read2st64_b32 v[86:87], v10 offset0:50 offset1:51\n"
  "ds_read2st64_b32 v[90:91], v10 offset0:52 offset1:53\n"
  "ds_read2st64_b32 v[94:95], v10 offset0:54 offset1:55\n"
  "ds_read2st64_b32 v[98:99], v10 offset0:56 offset1:57\n"
  "ds_read2st64_b32 v[102:103], v10 offset0:58 offset1:59\n"
  "ds_read2st64_b32 v[106:107], v10 offset0:60 offset1:61\n"
  "ds_read2st64_b32 v[110:111], v10 offset0:62 offset1:63\n"
  "s_waitcnt lgkmcnt(0)\n"
  "v_pk_add_f32 v[32:33], v[32:33], v[82:83]\n"
  "v_pk_add_f32 v[54:55], v[54:55], v[86:87]\n"
  "v_pk_add_f32 v[58:59], v[58:59], v[90:91]\n"
  "v_pk_add_f32 v[62:63], v[62:63], v[94:95]\n"
  "v_pk_add_f32 v[66:67], v[66:67], v[98:99]\n"
  "v_pk_add_f32 v[70:71], v[70:71], v[102:103]\n"
  "v_pk_add_f32 v[74:75], v[74:75], v[106:107]\n"
  "v_pk_add_f32 v[78:79], v[78:79], v[110:111]\n"
  "v_pk_add_f32 v[32:33], v[32:33], v[66:67]\n"
  "v_pk_add_f32 v[54:55], v[54:55], v[70:71]\n"
  "v_pk_add_f32 v[58:59], v[58:59], v[74:75]\n"
  "v_pk_add_f32 v[62:63], v[62:63], v[78:79]\n"
  "v_pk_add_f32 v[32:33], v[32:33], v[58:59]\n"
  "v_pk_add_f32 v[54:55], v[54:55], v[62:63]\n"
  "v_pk_add_f32 v[32:33], v[32:33], v[54:55]\n"
  "v_mul_f32 v32, 0x3b124925, v32\n"
  "v_mul_f32 v33, 0x3b124925, v33\n"
  "v_fma_f32 v33, -v32, v32, v33\n"
  "v_max_f32 v33, 0, v33\n"
  "v_cvt_pk_bf16_f32 v33, v33, v33\n"
  "v_lshlrev_b32 v33, 16, v33\n"
  "v_add_f32 v33, 0x3727c5ac, v33\n"
  "v_cvt_pk_bf16_f32 v33, v33, v33\n"
  "v_lshlrev_b32 v33, 16, v33\n"
  "v_sqrt_f32 v33, v33\n"
  "s_lshl_b32 s4, s43, 3\n"
  "s_add_u32 s4, s4, 163840\n"
  "v_lshl_add_u32 v10, v2, 3, s4\n"
  "v_cvt_pk_bf16_f32 v32, v32, v33\n"
  "v_mov_b32 v33, s8\n"
  "buffer_store_dwordx2 v[32:33], v10, s[24:27], 0 offen sc1\n"
  "L9_%=:\n"
  "s_endpgm\n"
  "L13_%=:\n"
  "s_sub_u32 s19, s19, 1\n"
  "s_cbranch_scc1 L14_%=\n"
  "buffer_load_dwordx4 v[14:17], v38, s[24:27], 0 offen sc1\n"
  "buffer_load_dwordx4 v[80:83], v39, s[24:27], 0 offen sc1\n"
  "buffer_load_dwordx4 v[20:23], v38, s[24:27], s22 offen sc1\n"
  "buffer_load_dwordx4 v[84:87], v39, s[24:27], s22 offen sc1\n"
  "buffer_load_dwordx4 v[50:53], v38, s[24:27], s23 offen sc1\n"
  "buffer_load_dwordx4 v[88:91], v39, s[24:27], s23 offen sc1\n"
  "buffer_load_dwordx4 v[76:79], v38, s[24:27], s43 offen sc1\n"
  "buffer_load_dwordx4 v[92:95], v39, s[24:27], s43 offen sc1\n"
  "s_waitcnt vmcnt(0)\n"
  "s_branch L12_%=\n"
  "L15_%=:\n"
  "v_mov_b32 v10, s8\n"
  "v_mov_b32 v11, 0\n"
  "s_mov_b64 exec, 1\n"
  "buffer_store_dword v10, v11, s[24:27], 0 offen\n"
  "s_mov_b64 exec, -1\n"
  "s_branch L16_%=\n"
  "L18_%=:\n"
  "s_sub_u32 s19, s19, 1\n"
  "s_cbranch_scc1 L19_%=\n"
  "buffer_load_dword v5, v45, s[24:27], 0 offen offset:0 sc1\n"
  "s_waitcnt vmcnt(0)\n"
  "s_branch L17_%=\n"
  "L21_%=:\n"
  "s_sub_u32 s19, s19, 1\n"
  "s_cbranch_scc1 L22_%=\n"
  "buffer_load_dwordx2 v[14:15], v6, s[24:27], 0 offen sc1\n"
  "buffer_load_dwordx2 v[16:17], v6, s[24:27], s46 offen sc1\n"
  "s_waitcnt vmcnt(0)\n"
  "s_branch L20_%=\n"
  "L24_%=:\n"
  "s_sub_u32 s19, s19, 1\n"
  "s_cbranch_scc1 L25_%=\n"
  "buffer_load_dword v5, v45, s[24:27], 0 offen offset:128 sc1\n"
  "s_waitcnt vmcnt(0)\n"
  "s_branch L23_%=\n"
  "L27_%=:\n"
  "s_sub_u32 s19, s19, 1\n"
  "s_cbranch_scc1 L28_%=\n"
  "buffer_load_dwordx2 v[32:33], v7, s[24:27], 0 offen sc1\n"
  "buffer_load_dwordx2 v[34:35], v7, s[24:27], 0 offen offset:256 sc1\n"
  "s_waitcnt vmcnt(0)\n"
  "s_branch L26_%=\n"
  "L30_%=:\n"
  "s_sub_u32 s19, s19, 1\n"
  "s_cbranch_scc1 L31_%=\n"
  "buffer_load_dwordx2 v[32:33], v7, s[24:27], 0 offen sc1\n"
  "buffer_load_dwordx2 v[34:35], v7, s[24:27], 0 offen offset:256 sc1\n"
  "s_waitcnt vmcnt(0)\n"
  "s_branch L29_%=\n"
  "L32_%=:\n"
  "s_waitcnt vmcnt(0)\n"
  "v_subrev_u32 v11, 114688, v56\n"
  "s_nop 7\n"
  "L34_%=:\n"
  "buffer_load_dwordx4 v[14:17], v11, s[24:27], s53 offen sc1\n"
  "buffer_load_dwordx4 v[216:219], v11, s[24:27], s58 offen sc1\n"
  "buffer_load_dwordx4 v[220:223], v11, s[24:27], s59 offen sc1\n"
  "buffer_load_dwordx4 v[224:227], v11, s[24:27], s60 offen sc1\n"
  "buffer_load_dwordx4 v[38:41], v11, s[24:27], s61 offen sc1\n"
  "buffer_load_dwordx4 v[42:45], v11, s[24:27], s63 offen sc1\n"
  "buffer_load_dwordx4 v[64:67], v11, s[24:27], s64 offen sc1\n"
  "buffer_load_dwordx4 v[68:71], v11, s[24:27], s65 offen sc1\n"
  "buffer_load_dwordx4 v[72:75], v11, s[24:27], s66 offen sc1\n"
  "buffer_load_dwordx4 v[76:79], v11, s[24:27], s67 offen sc1\n"
  "buffer_load_dwordx4 v[96:99], v11, s[24:27], s68 offen sc1\n"
  "buffer_load_dwordx4 v[100:103], v11, s[24:27], s69 offen sc1\n"
  "buffer_load_dwordx4 v[104:107], v11, s[24:27], s70 offen sc1\n"
  "buffer_load_dwordx4 v[108:111], v11, s[24:27], s71 offen sc1\n"
  "s_waitcnt vmcnt(0)\n"
  "v_max3_u32 v7, v14, v15, 0\n"
  "v_max3_u32 v7, v16, v17, v7\n"
  "v_max3_u32 v7, v216, v217, v7\n"
  "v_max3_u32 v7, v218, v219, v7\n"
  "v_max3_u32 v7, v220, v221, v7\n"
  "v_max3_u32 v7, v222, v223, v7\n"
  "v_max3_u32 v7, v224, v225, v7\n"
  "v_max3_u32 v7, v226, v227, v7\n"
  "v_max3_u32 v7, v38, v39, v7\n"
  "v_max3_u32 v7, v40, v41, v7\n"
  "v_max3_u32 v7, v42, v43, v7\n"
  "v_max3_u32 v7, v44, v45, v7\n"
  "v_max3_u32 v7, v64, v65, v7\n"
  "v_max3_u32 v7, v66, v67, v7\n"
  "v_max3_u32 v7, v68, v69, v7\n"
  "v_max3_u32 v7, v70, v71, v7\n"
  "v_max3_u32 v7, v72, v73, v7\n"
  "v_max3_u32 v7, v74, v75, v7\n"
  "v_max3_u32 v7, v76, v77, v7\n"
  "v_max3_u32 v7, v78, v79, v7\n"
  "v_max3_u32 v7, v96, v97, v7\n"
  "v_max3_u32 v7, v98, v99, v7\n"
  "v_max3_u32 v7, v100, v101, v7\n"
  "v_max3_u32 v7, v102, v103, v7\n"
  "v_max3_u32 v7, v104, v105, v7\n"
  "v_max3_u32 v7, v106, v107, v7\n"
  "v_max3_u32 v7, v108, v109, v7\n"
  "v_max3_u32 v7, v110, v111, v7\n"
  "v_cmp_eq_u32 vcc, -1, v7\n"
  "s_nop 0\n"
  "s_and_b64 vcc, exec, vcc\n"
  "s_cbranch_vccz L35_%=\n"
  "s_sub_u32 s19, s19, 1\n"
  "s_cbranch_scc0 L34_%=\n"
  "L35_%=:\n"
  "v_mfma_f32_16x16x32_bf16 v[18:21], a[224:227], v[14:17], 0\n"
  "v_mfma_f32_16x16x32_bf16 v[22:25], a[228:231], v[14:17], 0\n"
  "v_mfma_f32_16x16x32_bf16 v[26:29], a[232:235], v[14:17], 0\n"
  "v_mfma_f32_16x16x32_bf16 v[30:33], a[236:239], v[14:17], 0\n"
  "v_mfma_f32_16x16x32_bf16 v[18:21], a[240:243], v[216:219], v[18:21]\n"
  "v_mfma_f32_16x16x32_bf16 v[22:25], a[244:247], v[216:219], v[22:25]\n"
  "v_mfma_f32_16x16x32_bf16 v[26:29], a[248:251], v[216:219], v[26:29]\n"
  "v_mfma_f32_16x16x32_bf16 v[30:33], a[252:255], v[216:219], v[30:33]\n"
  "v_mfma_f32_16x16x32_bf16 v[18:21], v[152:155], v[220:223], v[18:21]\n"
  "v_mfma_f32_16x16x32_bf16 v[22:25], v[156:159], v[220:223], v[22:25]\n"
  "v_mfma_f32_16x16x32_bf16 v[26:29], v[160:163], v[220:223], v[26:29]\n"
  "v_mfma_f32_16x16x32_bf16 v[30:33], v[164:167], v[220:223], v[30:33]\n"
  "v_mfma_f32_16x16x32_bf16 v[18:21], v[168:171], v[224:227], v[18:21]\n"
  "v_mfma_f32_16x16x32_bf16 v[22:25], v[172:175], v[224:227], v[22:25]\n"
  "v_mfma_f32_16x16x32_bf16 v[26:29], v[176:179], v[224:227], v[26:29]\n"
  "v_mfma_f32_16x16x32_bf16 v[30:33], v[180:183], v[224:227], v[30:33]\n"
  "v_mfma_f32_16x16x32_bf16 v[18:21], v[184:187], v[38:41], v[18:21]\n"
  "v_mfma_f32_16x16x32_bf16 v[22:25], v[188:191], v[38:41], v[22:25]\n"
  "v_mfma_f32_16x16x32_bf16 v[26:29], v[192:195], v[38:41], v[26:29]\n"
  "v_mfma_f32_16x16x32_bf16 v[30:33], v[196:199], v[38:41], v[30:33]\n"
  "v_mfma_f32_16x16x32_bf16 v[18:21], v[200:203], v[42:45], v[18:21]\n"
  "v_mfma_f32_16x16x32_bf16 v[22:25], v[204:207], v[42:45], v[22:25]\n"
  "v_mfma_f32_16x16x32_bf16 v[26:29], v[208:211], v[42:45], v[26:29]\n"
  "v_mfma_f32_16x16x32_bf16 v[30:33], v[212:215], v[42:45], v[30:33]\n"
  "v_mfma_f32_16x16x32_bf16 v[18:21], a[0:3], v[64:67], v[18:21]\n"
  "v_mfma_f32_16x16x32_bf16 v[22:25], a[4:7], v[64:67], v[22:25]\n"
  "v_mfma_f32_16x16x32_bf16 v[26:29], a[8:11], v[64:67], v[26:29]\n"
  "v_mfma_f32_16x16x32_bf16 v[30:33], a[12:15], v[64:67], v[30:33]\n"
  "v_mfma_f32_16x16x32_bf16 v[18:21], a[16:19], v[68:71], v[18:21]\n"
  "v_mfma_f32_16x16x32_bf16 v[22:25], a[20:23], v[68:71], v[22:25]\n"
  "v_mfma_f32_16x16x32_bf16 v[26:29], a[24:27], v[68:71], v[26:29]\n"
  "v_mfma_f32_16x16x32_bf16 v[30:33], a[28:31], v[68:71], v[30:33]\n"
  "v_mfma_f32_16x16x32_bf16 v[18:21], a[32:35], v[72:75], v[18:21]\n"
  "v_mfma_f32_16x16x32_bf16 v[22:25], a[36:39], v[72:75], v[22:25]\n"
  "v_mfma_f32_16x16x32_bf16 v[26:29], a[40:43], v[72:75], v[26:29]\n"
  "v_mfma_f32_16x16x32_bf16 v[30:33], a[44:47], v[72:75], v[30:33]\n"
  "v_mfma_f32_16x16x32_bf16 v[18:21], a[48:51], v[76:79], v[18:21]\n"
  "v_mfma_f32_16x16x32_bf16 v[22:25], a[52:55], v[76:79], v[22:25]\n"
  "v_mfma_f32_16x16x32_bf16 v[26:29], a[56:59], v[76:79], v[26:29]\n"
  "v_mfma_f32_16x16x32_bf16 v[30:33], a[60:63], v[76:79], v[30:33]\n"
  "v_mfma_f32_16x16x32_bf16 v[18:21], a[64:67], v[96:99], v[18:21]\n"
  "v_mfma_f32_16x16x32_bf16 v[22:25], a[68:71], v[96:99], v[22:25]\n"
  "v_mfma_f32_16x16x32_bf16 v[26:29], a[72:75], v[96:99], v[26:29]\n"
  "v_mfma_f32_16x16x32_bf16 v[30:33], a[76:79], v[96:99], v[30:33]\n"
  "v_mfma_f32_16x16x32_bf16 v[18:21], a[80:83], v[100:103], v[18:21]\n"
  "v_mfma_f32_16x16x32_bf16 v[22:25], a[84:87], v[100:103], v[22:25]\n"
  "v_mfma_f32_16x16x32_bf16 v[26:29], a[88:91], v[100:103], v[26:29]\n"
  "v_mfma_f32_16x16x32_bf16 v[30:33], a[92:95], v[100:103], v[30:33]\n"
  "v_mfma_f32_16x16x32_bf16 v[18:21], a[96:99], v[104:107], v[18:21]\n"
  "v_mfma_f32_16x16x32_bf16 v[22:25], a[100:103], v[104:107], v[22:25]\n"
  "v_mfma_f32_16x16x32_bf16 v[26:29], a[104:107], v[104:107], v[26:29]\n"
  "v_mfma_f32_16x16x32_bf16 v[30:33], a[108:111], v[104:107], v[30:33]\n"
  "v_mfma_f32_16x16x32_bf16 v[18:21], a[112:115], v[108:111], v[18:21]\n"
  "v_mfma_f32_16x16x32_bf16 v[22:25], a[116:119], v[108:111], v[22:25]\n"
  "v_mfma_f32_16x16x32_bf16 v[26:29], a[120:123], v[108:111], v[26:29]\n"
  "v_mfma_f32_16x16x32_bf16 v[30:33], a[124:127], v[108:111], v[30:33]\n"
  "s_nop 7\n"
  "buffer_load_dwordx4 v[14:17], v56, s[24:27], s53 offen sc1\n"
  "buffer_load_dwordx4 v[216:219], v56, s[24:27], s58 offen sc1\n"
  "buffer_load_dwordx4 v[220:223], v56, s[24:27], s59 offen sc1\n"
  "buffer_load_dwordx4 v[224:227], v56, s[24:27], s60 offen sc1\n"
  "buffer_load_dwordx4 v[38:41], v56, s[24:27], s61 offen sc1\n"
  "buffer_load_dwordx4 v[42:45], v56, s[24:27], s63 offen sc1\n"
  "buffer_load_dwordx4 v[64:67], v56, s[24:27], s64 offen sc1\n"
  "buffer_load_dwordx4 v[68:71], v56, s[24:27], s65 offen sc1\n"
  "buffer_load_dwordx4 v[72:75], v56, s[24:27], s66 offen sc1\n"
  "buffer_load_dwordx4 v[76:79], v56, s[24:27], s67 offen sc1\n"
  "buffer_load_dwordx4 v[96:99], v56, s[24:27], s68 offen sc1\n"
  "buffer_load_dwordx4 v[100:103], v56, s[24:27], s69 offen sc1\n"
  "buffer_load_dwordx4 v[104:107], v56, s[24:27], s70 offen sc1\n"
  "buffer_load_dwordx4 v[108:111], v56, s[24:27], s71 offen sc1\n"
  "s_waitcnt vmcnt(0)\n"
  "s_branch L33_%=\n"
  "L36_%=:\n"
  "s_waitcnt vmcnt(0)\n"
  "v_subrev_u32 v11, 114688, v56\n"
  "s_nop 7\n"
  "L38_%=:\n"
  "buffer_load_dwordx4 v[14:17], v11, s[24:27], s53 offen sc1\n"
  "buffer_load_dwordx4 v[216:219], v11, s[24:27], s58 offen sc1\n"
  "buffer_load_dwordx4 v[220:223], v11, s[24:27], s59 offen sc1\n"
  "buffer_load_dwordx4 v[224:227], v11, s[24:27], s60 offen sc1\n"
  "buffer_load_dwordx4 v[38:41], v11, s[24:27], s61 offen sc1\n"
  "buffer_load_dwordx4 v[42:45], v11, s[24:27], s63 offen sc1\n"
  "buffer_load_dwordx4 v[64:67], v11, s[24:27], s64 offen sc1\n"
  "buffer_load_dwordx4 v[68:71], v11, s[24:27], s65 offen sc1\n"
  "buffer_load_dwordx4 v[72:75], v11, s[24:27], s66 offen sc1\n"
  "buffer_load_dwordx4 v[76:79], v11, s[24:27], s67 offen sc1\n"
  "buffer_load_dwordx4 v[96:99], v11, s[24:27], s68 offen sc1\n"
  "buffer_load_dwordx4 v[100:103], v11, s[24:27], s69 offen sc1\n"
  "buffer_load_dwordx4 v[104:107], v11, s[24:27], s70 offen sc1\n"
  "buffer_load_dwordx4 v[108:111], v11, s[24:27], s71 offen sc1\n"
  "s_waitcnt vmcnt(0)\n"
  "v_max3_u32 v7, v14, v15, 0\n"
  "v_max3_u32 v7, v16, v17, v7\n"
  "v_max3_u32 v7, v216, v217, v7\n"
  "v_max3_u32 v7, v218, v219, v7\n"
  "v_max3_u32 v7, v220, v221, v7\n"
  "v_max3_u32 v7, v222, v223, v7\n"
  "v_max3_u32 v7, v224, v225, v7\n"
  "v_max3_u32 v7, v226, v227, v7\n"
  "v_max3_u32 v7, v38, v39, v7\n"
  "v_max3_u32 v7, v40, v41, v7\n"
  "v_max3_u32 v7, v42, v43, v7\n"
  "v_max3_u32 v7, v44, v45, v7\n"
  "v_max3_u32 v7, v64, v65, v7\n"
  "v_max3_u32 v7, v66, v67, v7\n"
  "v_max3_u32 v7, v68, v69, v7\n"
  "v_max3_u32 v7, v70, v71, v7\n"
  "v_max3_u32 v7, v72, v73, v7\n"
  "v_max3_u32 v7, v74, v75, v7\n"
  "v_max3_u32 v7, v76, v77, v7\n"
  "v_max3_u32 v7, v78, v79, v7\n"
  "v_max3_u32 v7, v96, v97, v7\n"
  "v_max3_u32 v7, v98, v99, v7\n"
  "v_max3_u32 v7, v100, v101, v7\n"
  "v_max3_u32 v7, v102, v103, v7\n"
  "v_max3_u32 v7, v104, v105, v7\n"
  "v_max3_u32 v7, v106, v107, v7\n"
  "v_max3_u32 v7, v108, v109, v7\n"
  "v_max3_u32 v7, v110, v111, v7\n"
  "v_cmp_eq_u32 vcc, -1, v7\n"
  "s_nop 0\n"
  "s_and_b64 vcc, exec, vcc\n"
  "s_cbranch_vccz L39_%=\n"
  "s_sub_u32 s19, s19, 1\n"
  "s_cbranch_scc0 L38_%=\n"
  "L39_%=:\n"
  "v_mfma_f32_16x16x32_bf16 v[80:83], a[224:227], v[14:17], 0\n"
  "v_mfma_f32_16x16x32_bf16 v[84:87], a[228:231], v[14:17], 0\n"
  "v_mfma_f32_16x16x32_bf16 v[88:91], a[232:235], v[14:17], 0\n"
  "v_mfma_f32_16x16x32_bf16 v[92:95], a[236:239], v[14:17], 0\n"
  "v_mfma_f32_16x16x32_bf16 v[80:83], a[240:243], v[216:219], v[80:83]\n"
  "v_mfma_f32_16x16x32_bf16 v[84:87], a[244:247], v[216:219], v[84:87]\n"
  "v_mfma_f32_16x16x32_bf16 v[88:91], a[248:251], v[216:219], v[88:91]\n"
  "v_mfma_f32_16x16x32_bf16 v[92:95], a[252:255], v[216:219], v[92:95]\n"
  "v_mfma_f32_16x16x32_bf16 v[80:83], v[152:155], v[220:223], v[80:83]\n"
  "v_mfma_f32_16x16x32_bf16 v[84:87], v[156:159], v[220:223], v[84:87]\n"
  "v_mfma_f32_16x16x32_bf16 v[88:91], v[160:163], v[220:223], v[88:91]\n"
  "v_mfma_f32_16x16x32_bf16 v[92:95], v[164:167], v[220:223], v[92:95]\n"
  "v_mfma_f32_16x16x32_bf16 v[80:83], v[168:171], v[224:227], v[80:83]\n"
  "v_mfma_f32_16x16x32_bf16 v[84:87], v[172:175], v[224:227], v[84:87]\n"
  "v_mfma_f32_16x16x32_bf16 v[88:91], v[176:179], v[224:227], v[88:91]\n"
  "v_mfma_f32_16x16x32_bf16 v[92:95], v[180:183], v[224:227], v[92:95]\n"
  "v_mfma_f32_16x16x32_bf16 v[80:83], v[184:187], v[38:41], v[80:83]\n"
  "v_mfma_f32_16x16x32_bf16 v[84:87], v[188:191], v[38:41], v[84:87]\n"
  "v_mfma_f32_16x16x32_bf16 v[88:91], v[192:195], v[38:41], v[88:91]\n"
  "v_mfma_f32_16x16x32_bf16 v[92:95], v[196:199], v[38:41], v[92:95]\n"
  "v_mfma_f32_16x16x32_bf16 v[80:83], v[200:203], v[42:45], v[80:83]\n"
  "v_mfma_f32_16x16x32_bf16 v[84:87], v[204:207], v[42:45], v[84:87]\n"
  "v_mfma_f32_16x16x32_bf16 v[88:91], v[208:211], v[42:45], v[88:91]\n"
  "v_mfma_f32_16x16x32_bf16 v[92:95], v[212:215], v[42:45], v[92:95]\n"
  "v_mfma_f32_16x16x32_bf16 v[80:83], a[0:3], v[64:67], v[80:83]\n"
  "v_mfma_f32_16x16x32_bf16 v[84:87], a[4:7], v[64:67], v[84:87]\n"
  "v_mfma_f32_16x16x32_bf16 v[88:91], a[8:11], v[64:67], v[88:91]\n"
  "v_mfma_f32_16x16x32_bf16 v[92:95], a[12:15], v[64:67], v[92:95]\n"
  "v_mfma_f32_16x16x32_bf16 v[80:83], a[16:19], v[68:71], v[80:83]\n"
  "v_mfma_f32_16x16x32_bf16 v[84:87], a[20:23], v[68:71], v[84:87]\n"
  "v_mfma_f32_16x16x32_bf16 v[88:91], a[24:27], v[68:71], v[88:91]\n"
  "v_mfma_f32_16x16x32_bf16 v[92:95], a[28:31], v[68:71], v[92:95]\n"
  "v_mfma_f32_16x16x32_bf16 v[80:83], a[32:35], v[72:75], v[80:83]\n"
  "v_mfma_f32_16x16x32_bf16 v[84:87], a[36:39], v[72:75], v[84:87]\n"
  "v_mfma_f32_16x16x32_bf16 v[88:91], a[40:43], v[72:75], v[88:91]\n"
  "v_mfma_f32_16x16x32_bf16 v[92:95], a[44:47], v[72:75], v[92:95]\n"
  "v_mfma_f32_16x16x32_bf16 v[80:83], a[48:51], v[76:79], v[80:83]\n"
  "v_mfma_f32_16x16x32_bf16 v[84:87], a[52:55], v[76:79], v[84:87]\n"
  "v_mfma_f32_16x16x32_bf16 v[88:91], a[56:59], v[76:79], v[88:91]\n"
  "v_mfma_f32_16x16x32_bf16 v[92:95], a[60:63], v[76:79], v[92:95]\n"
  "v_mfma_f32_16x16x32_bf16 v[80:83], a[64:67], v[96:99], v[80:83]\n"
  "v_mfma_f32_16x16x32_bf16 v[84:87], a[68:71], v[96:99], v[84:87]\n"
  "v_mfma_f32_16x16x32_bf16 v[88:91], a[72:75], v[96:99], v[88:91]\n"
  "v_mfma_f32_16x16x32_bf16 v[92:95], a[76:79], v[96:99], v[92:95]\n"
  "v_mfma_f32_16x16x32_bf16 v[80:83], a[80:83], v[100:103], v[80:83]\n"
  "v_mfma_f32_16x16x32_bf16 v[84:87], a[84:87], v[100:103], v[84:87]\n"
  "v_mfma_f32_16x16x32_bf16 v[88:91], a[88:91], v[100:103], v[88:91]\n"
  "v_mfma_f32_16x16x32_bf16 v[92:95], a[92:95], v[100:103], v[92:95]\n"
  "v_mfma_f32_16x16x32_bf16 v[80:83], a[96:99], v[104:107], v[80:83]\n"
  "v_mfma_f32_16x16x32_bf16 v[84:87], a[100:103], v[104:107], v[84:87]\n"
  "v_mfma_f32_16x16x32_bf16 v[88:91], a[104:107], v[104:107], v[88:91]\n"
  "v_mfma_f32_16x16x32_bf16 v[92:95], a[108:111], v[104:107], v[92:95]\n"
  "v_mfma_f32_16x16x32_bf16 v[80:83], a[112:115], v[108:111], v[80:83]\n"
  "v_mfma_f32_16x16x32_bf16 v[84:87], a[116:119], v[108:111], v[84:87]\n"
  "v_mfma_f32_16x16x32_bf16 v[88:91], a[120:123], v[108:111], v[88:91]\n"
  "v_mfma_f32_16x16x32_bf16 v[92:95], a[124:127], v[108:111], v[92:95]\n"
  "s_nop 7\n"
  "buffer_load_dwordx4 v[14:17], v56, s[24:27], s53 offen sc1\n"
  "buffer_load_dwordx4 v[216:219], v56, s[24:27], s58 offen sc1\n"
  "buffer_load_dwordx4 v[220:223], v56, s[24:27], s59 offen sc1\n"
  "buffer_load_dwordx4 v[224:227], v56, s[24:27], s60 offen sc1\n"
  "buffer_load_dwordx4 v[38:41], v56, s[24:27], s61 offen sc1\n"
  "buffer_load_dwordx4 v[42:45], v56, s[24:27], s63 offen sc1\n"
  "buffer_load_dwordx4 v[64:67], v56, s[24:27], s64 offen sc1\n"
  "buffer_load_dwordx4 v[68:71], v56, s[24:27], s65 offen sc1\n"
  "buffer_load_dwordx4 v[72:75], v56, s[24:27], s66 offen sc1\n"
  "buffer_load_dwordx4 v[76:79], v56, s[24:27], s67 offen sc1\n"
  "buffer_load_dwordx4 v[96:99], v56, s[24:27], s68 offen sc1\n"
  "buffer_load_dwordx4 v[100:103], v56, s[24:27], s69 offen sc1\n"
  "buffer_load_dwordx4 v[104:107], v56, s[24:27], s70 offen sc1\n"
  "buffer_load_dwordx4 v[108:111], v56, s[24:27], s71 offen sc1\n"
  "s_waitcnt vmcnt(0)\n"
  "s_branch L37_%=\n"
  "L40_%=:\n"
  "s_waitcnt vmcnt(0)\n"
  "v_subrev_u32 v11, 114688, v56\n"
  "s_nop 7\n"
  "L42_%=:\n"
  "buffer_load_dwordx4 v[14:17], v11, s[24:27], s53 offen sc1\n"
  "buffer_load_dwordx4 v[216:219], v11, s[24:27], s58 offen sc1\n"
  "buffer_load_dwordx4 v[220:223], v11, s[24:27], s59 offen sc1\n"
  "buffer_load_dwordx4 v[224:227], v11, s[24:27], s60 offen sc1\n"
  "buffer_load_dwordx4 v[38:41], v11, s[24:27], s61 offen sc1\n"
  "buffer_load_dwordx4 v[42:45], v11, s[24:27], s63 offen sc1\n"
  "buffer_load_dwordx4 v[64:67], v11, s[24:27], s64 offen sc1\n"
  "buffer_load_dwordx4 v[68:71], v11, s[24:27], s65 offen sc1\n"
  "buffer_load_dwordx4 v[72:75], v11, s[24:27], s66 offen sc1\n"
  "buffer_load_dwordx4 v[76:79], v11, s[24:27], s67 offen sc1\n"
  "buffer_load_dwordx4 v[96:99], v11, s[24:27], s68 offen sc1\n"
  "buffer_load_dwordx4 v[100:103], v11, s[24:27], s69 offen sc1\n"
  "buffer_load_dwordx4 v[104:107], v11, s[24:27], s70 offen sc1\n"
  "buffer_load_dwordx4 v[108:111], v11, s[24:27], s71 offen sc1\n"
  "s_waitcnt vmcnt(0)\n"
  "v_max3_u32 v7, v14, v15, 0\n"
  "v_max3_u32 v7, v16, v17, v7\n"
  "v_max3_u32 v7, v216, v217, v7\n"
  "v_max3_u32 v7, v218, v219, v7\n"
  "v_max3_u32 v7, v220, v221, v7\n"
  "v_max3_u32 v7, v222, v223, v7\n"
  "v_max3_u32 v7, v224, v225, v7\n"
  "v_max3_u32 v7, v226, v227, v7\n"
  "v_max3_u32 v7, v38, v39, v7\n"
  "v_max3_u32 v7, v40, v41, v7\n"
  "v_max3_u32 v7, v42, v43, v7\n"
  "v_max3_u32 v7, v44, v45, v7\n"
  "v_max3_u32 v7, v64, v65, v7\n"
  "v_max3_u32 v7, v66, v67, v7\n"
  "v_max3_u32 v7, v68, v69, v7\n"
  "v_max3_u32 v7, v70, v71, v7\n"
  "v_max3_u32 v7, v72, v73, v7\n"
  "v_max3_u32 v7, v74, v75, v7\n"
  "v_max3_u32 v7, v76, v77, v7\n"
  "v_max3_u32 v7, v78, v79, v7\n"
  "v_max3_u32 v7, v96, v97, v7\n"
  "v_max3_u32 v7, v98, v99, v7\n"
  "v_max3_u32 v7, v100, v101, v7\n"
  "v_max3_u32 v7, v102, v103, v7\n"
  "v_max3_u32 v7, v104, v105, v7\n"
  "v_max3_u32 v7, v106, v107, v7\n"
  "v_max3_u32 v7, v108, v109, v7\n"
  "v_max3_u32 v7, v110, v111, v7\n"
  "v_cmp_eq_u32 vcc, -1, v7\n"
  "s_nop 0\n"
  "s_and_b64 vcc, exec, vcc\n"
  "s_cbranch_vccz L43_%=\n"
  "s_sub_u32 s19, s19, 1\n"
  "s_cbranch_scc0 L42_%=\n"
  "L43_%=:\n"
  "v_mfma_f32_16x16x32_bf16 v[112:115], a[224:227], v[14:17], 0\n"
  "v_mfma_f32_16x16x32_bf16 v[116:119], a[228:231], v[14:17], 0\n"
  "v_mfma_f32_16x16x32_bf16 v[120:123], a[232:235], v[14:17], 0\n"
  "v_mfma_f32_16x16x32_bf16 v[124:127], a[236:239], v[14:17], 0\n"
  "v_mfma_f32_16x16x32_bf16 v[112:115], a[240:243], v[216:219], v[112:115]\n"
  "v_mfma_f32_16x16x32_bf16 v[116:119], a[244:247], v[216:219], v[116:119]\n"
  "v_mfma_f32_16x16x32_bf16 v[120:123], a[248:251], v[216:219], v[120:123]\n"
  "v_mfma_f32_16x16x32_bf16 v[124:127], a[252:255], v[216:219], v[124:127]\n"
  "v_mfma_f32_16x16x32_bf16 v[112:115], v[152:155], v[220:223], v[112:115]\n"
  "v_mfma_f32_16x16x32_bf16 v[116:119], v[156:159], v[220:223], v[116:119]\n"
  "v_mfma_f32_16x16x32_bf16 v[120:123], v[160:163], v[220:223], v[120:123]\n"
  "v_mfma_f32_16x16x32_bf16 v[124:127], v[164:167], v[220:223], v[124:127]\n"
  "v_mfma_f32_16x16x32_bf16 v[112:115], v[168:171], v[224:227], v[112:115]\n"
  "v_mfma_f32_16x16x32_bf16 v[116:119], v[172:175], v[224:227], v[116:119]\n"
  "v_mfma_f32_16x16x32_bf16 v[120:123], v[176:179], v[224:227], v[120:123]\n"
  "v_mfma_f32_16x16x32_bf16 v[124:127], v[180:183], v[224:227], v[124:127]\n"
  "v_mfma_f32_16x16x32_bf16 v[112:115], v[184:187], v[38:41], v[112:115]\n"
  "v_mfma_f32_16x16x32_bf16 v[116:119], v[188:191], v[38:41], v[116:119]\n"
  "v_mfma_f32_16x16x32_bf16 v[120:123], v[192:195], v[38:41], v[120:123]\n"
  "v_mfma_f32_16x16x32_bf16 v[124:127], v[196:199], v[38:41], v[124:127]\n"
  "v_mfma_f32_16x16x32_bf16 v[112:115], v[200:203], v[42:45], v[112:115]\n"
  "v_mfma_f32_16x16x32_bf16 v[116:119], v[204:207], v[42:45], v[116:119]\n"
  "v_mfma_f32_16x16x32_bf16 v[120:123], v[208:211], v[42:45], v[120:123]\n"
  "v_mfma_f32_16x16x32_bf16 v[124:127], v[212:215], v[42:45], v[124:127]\n"
  "v_mfma_f32_16x16x32_bf16 v[112:115], a[0:3], v[64:67], v[112:115]\n"
  "v_mfma_f32_16x16x32_bf16 v[116:119], a[4:7], v[64:67], v[116:119]\n"
  "v_mfma_f32_16x16x32_bf16 v[120:123], a[8:11], v[64:67], v[120:123]\n"
  "v_mfma_f32_16x16x32_bf16 v[124:127], a[12:15], v[64:67], v[124:127]\n"
  "v_mfma_f32_16x16x32_bf16 v[112:115], a[16:19], v[68:71], v[112:115]\n"
  "v_mfma_f32_16x16x32_bf16 v[116:119], a[20:23], v[68:71], v[116:119]\n"
  "v_mfma_f32_16x16x32_bf16 v[120:123], a[24:27], v[68:71], v[120:123]\n"
  "v_mfma_f32_16x16x32_bf16 v[124:127], a[28:31], v[68:71], v[124:127]\n"
  "v_mfma_f32_16x16x32_bf16 v[112:115], a[32:35], v[72:75], v[112:115]\n"
  "v_mfma_f32_16x16x32_bf16 v[116:119], a[36:39], v[72:75], v[116:119]\n"
  "v_mfma_f32_16x16x32_bf16 v[120:123], a[40:43], v[72:75], v[120:123]\n"
  "v_mfma_f32_16x16x32_bf16 v[124:127], a[44:47], v[72:75], v[124:127]\n"
  "v_mfma_f32_16x16x32_bf16 v[112:115], a[48:51], v[76:79], v[112:115]\n"
  "v_mfma_f32_16x16x32_bf16 v[116:119], a[52:55], v[76:79], v[116:119]\n"
  "v_mfma_f32_16x16x32_bf16 v[120:123], a[56:59], v[76:79], v[120:123]\n"
  "v_mfma_f32_16x16x32_bf16 v[124:127], a[60:63], v[76:79], v[124:127]\n"
  "v_mfma_f32_16x16x32_bf16 v[112:115], a[64:67], v[96:99], v[112:115]\n"
  "v_mfma_f32_16x16x32_bf16 v[116:119], a[68:71], v[96:99], v[116:119]\n"
  "v_mfma_f32_16x16x32_bf16 v[120:123], a[72:75], v[96:99], v[120:123]\n"
  "v_mfma_f32_16x16x32_bf16 v[124:127], a[76:79], v[96:99], v[124:127]\n"
  "v_mfma_f32_16x16x32_bf16 v[112:115], a[80:83], v[100:103], v[112:115]\n"
  "v_mfma_f32_16x16x32_bf16 v[116:119], a[84:87], v[100:103], v[116:119]\n"
  "v_mfma_f32_16x16x32_bf16 v[120:123], a[88:91], v[100:103], v[120:123]\n"
  "v_mfma_f32_16x16x32_bf16 v[124:127], a[92:95], v[100:103], v[124:127]\n"
  "v_mfma_f32_16x16x32_bf16 v[112:115], a[96:99], v[104:107], v[112:115]\n"
  "v_mfma_f32_16x16x32_bf16 v[116:119], a[100:103], v[104:107], v[116:119]\n"
  "v_mfma_f32_16x16x32_bf16 v[120:123], a[104:107], v[104:107], v[120:123]\n"
  "v_mfma_f32_16x16x32_bf16 v[124:127], a[108:111], v[104:107], v[124:127]\n"
  "v_mfma_f32_16x16x32_bf16 v[112:115], a[112:115], v[108:111], v[112:115]\n"
  "v_mfma_f32_16x16x32_bf16 v[116:119], a[116:119], v[108:111], v[116:119]\n"
  "v_mfma_f32_16x16x32_bf16 v[120:123], a[120:123], v[108:111], v[120:123]\n"
  "v_mfma_f32_16x16x32_bf16 v[124:127], a[124:127], v[108:111], v[124:127]\n"
  "s_nop 7\n"
  "buffer_load_dwordx4 v[14:17], v56, s[24:27], s53 offen sc1\n"
  "buffer_load_dwordx4 v[216:219], v56, s[24:27], s58 offen sc1\n"
  "buffer_load_dwordx4 v[220:223], v56, s[24:27], s59 offen sc1\n"
  "buffer_load_dwordx4 v[224:227], v56, s[24:27], s60 offen sc1\n"
  "buffer_load_dwordx4 v[38:41], v56, s[24:27], s61 offen sc1\n"
  "buffer_load_dwordx4 v[42:45], v56, s[24:27], s63 offen sc1\n"
  "buffer_load_dwordx4 v[64:67], v56, s[24:27], s64 offen sc1\n"
  "buffer_load_dwordx4 v[68:71], v56, s[24:27], s65 offen sc1\n"
  "buffer_load_dwordx4 v[72:75], v56, s[24:27], s66 offen sc1\n"
  "buffer_load_dwordx4 v[76:79], v56, s[24:27], s67 offen sc1\n"
  "buffer_load_dwordx4 v[96:99], v56, s[24:27], s68 offen sc1\n"
  "buffer_load_dwordx4 v[100:103], v56, s[24:27], s69 offen sc1\n"
  "buffer_load_dwordx4 v[104:107], v56, s[24:27], s70 offen sc1\n"
  "buffer_load_dwordx4 v[108:111], v56, s[24:27], s71 offen sc1\n"
  "s_waitcnt vmcnt(0)\n"
  "s_branch L41_%=\n"
  "L44_%=:\n"
  "s_waitcnt vmcnt(0)\n"
  "v_subrev_u32 v11, 0, v56\n"
  "s_nop 7\n"
  "L46_%=:\n"
  "buffer_load_dwordx4 v[14:17], v11, s[24:27], s53 offen sc1\n"
  "buffer_load_dwordx4 v[216:219], v11, s[24:27], s58 offen sc1\n"
  "buffer_load_dwordx4 v[220:223], v11, s[24:27], s59 offen sc1\n"
  "buffer_load_dwordx4 v[224:227], v11, s[24:27], s60 offen sc1\n"
  "buffer_load_dwordx4 v[38:41], v11, s[24:27], s61 offen sc1\n"
  "buffer_load_dwordx4 v[42:45], v11, s[24:27], s63 offen sc1\n"
  "buffer_load_dwordx4 v[64:67], v11, s[24:27], s64 offen sc1\n"
  "buffer_load_dwordx4 v[68:71], v11, s[24:27], s65 offen sc1\n"
  "buffer_load_dwordx4 v[72:75], v11, s[24:27], s66 offen sc1\n"
  "buffer_load_dwordx4 v[76:79], v11, s[24:27], s67 offen sc1\n"
  "buffer_load_dwordx4 v[96:99], v11, s[24:27], s68 offen sc1\n"
  "buffer_load_dwordx4 v[100:103], v11, s[24:27], s69 offen sc1\n"
  "buffer_load_dwordx4 v[104:107], v11, s[24:27], s70 offen sc1\n"
  "buffer_load_dwordx4 v[108:111], v11, s[24:27], s71 offen sc1\n"
  "s_waitcnt vmcnt(0)\n"
  "v_max3_u32 v7, v14, v15, 0\n"
  "v_max3_u32 v7, v16, v17, v7\n"
  "v_max3_u32 v7, v216, v217, v7\n"
  "v_max3_u32 v7, v218, v219, v7\n"
  "v_max3_u32 v7, v220, v221, v7\n"
  "v_max3_u32 v7, v222, v223, v7\n"
  "v_max3_u32 v7, v224, v225, v7\n"
  "v_max3_u32 v7, v226, v227, v7\n"
  "v_max3_u32 v7, v38, v39, v7\n"
  "v_max3_u32 v7, v40, v41, v7\n"
  "v_max3_u32 v7, v42, v43, v7\n"
  "v_max3_u32 v7, v44, v45, v7\n"
  "v_max3_u32 v7, v64, v65, v7\n"
  "v_max3_u32 v7, v66, v67, v7\n"
  "v_max3_u32 v7, v68, v69, v7\n"
  "v_max3_u32 v7, v70, v71, v7\n"
  "v_max3_u32 v7, v72, v73, v7\n"
  "v_max3_u32 v7, v74, v75, v7\n"
  "v_max3_u32 v7, v76, v77, v7\n"
  "v_max3_u32 v7, v78, v79, v7\n"
  "v_max3_u32 v7, v96, v97, v7\n"
  "v_max3_u32 v7, v98, v99, v7\n"
  "v_max3_u32 v7, v100, v101, v7\n"
  "v_max3_u32 v7, v102, v103, v7\n"
  "v_max3_u32 v7, v104, v105, v7\n"
  "v_max3_u32 v7, v106, v107, v7\n"
  "v_max3_u32 v7, v108, v109, v7\n"
  "v_max3_u32 v7, v110, v111, v7\n"
  "v_cmp_eq_u32 vcc, -1, v7\n"
  "s_nop 0\n"
  "s_and_b64 vcc, exec, vcc\n"
  "s_cbranch_vccz L47_%=\n"
  "s_sub_u32 s19, s19, 1\n"
  "s_cbranch_scc0 L46_%=\n"
  "L47_%=:\n"
  "v_mfma_f32_16x16x32_bf16 v[128:131], a[224:227], v[14:17], 0\n"
  "v_mfma_f32_16x16x32_bf16 v[132:135], a[228:231], v[14:17], 0\n"
  "v_mfma_f32_16x16x32_bf16 v[136:139], a[232:235], v[14:17], 0\n"
  "v_mfma_f32_16x16x32_bf16 v[140:143], a[236:239], v[14:17], 0\n"
  "v_mfma_f32_16x16x32_bf16 v[128:131], a[240:243], v[216:219], v[128:131]\n"
  "v_mfma_f32_16x16x32_bf16 v[132:135], a[244:247], v[216:219], v[132:135]\n"
  "v_mfma_f32_16x16x32_bf16 v[136:139], a[248:251], v[216:219], v[136:139]\n"
  "v_mfma_f32_16x16x32_bf16 v[140:143], a[252:255], v[216:219], v[140:143]\n"
  "v_mfma_f32_16x16x32_bf16 v[128:131], v[152:155], v[220:223], v[128:131]\n"
  "v_mfma_f32_16x16x32_bf16 v[132:135], v[156:159], v[220:223], v[132:135]\n"
  "v_mfma_f32_16x16x32_bf16 v[136:139], v[160:163], v[220:223], v[136:139]\n"
  "v_mfma_f32_16x16x32_bf16 v[140:143], v[164:167], v[220:223], v[140:143]\n"
  "v_mfma_f32_16x16x32_bf16 v[128:131], v[168:171], v[224:227], v[128:131]\n"
  "v_mfma_f32_16x16x32_bf16 v[132:135], v[172:175], v[224:227], v[132:135]\n"
  "v_mfma_f32_16x16x32_bf16 v[136:139], v[176:179], v[224:227], v[136:139]\n"
  "v_mfma_f32_16x16x32_bf16 v[140:143], v[180:183], v[224:227], v[140:143]\n"
  "v_mfma_f32_16x16x32_bf16 v[128:131], v[184:187], v[38:41], v[128:131]\n"
  "v_mfma_f32_16x16x32_bf16 v[132:135], v[188:191], v[38:41], v[132:135]\n"
  "v_mfma_f32_16x16x32_bf16 v[136:139], v[192:195], v[38:41], v[136:139]\n"
  "v_mfma_f32_16x16x32_bf16 v[140:143], v[196:199], v[38:41], v[140:143]\n"
  "v_mfma_f32_16x16x32_bf16 v[128:131], v[200:203], v[42:45], v[128:131]\n"
  "v_mfma_f32_16x16x32_bf16 v[132:135], v[204:207], v[42:45], v[132:135]\n"
  "v_mfma_f32_16x16x32_bf16 v[136:139], v[208:211], v[42:45], v[136:139]\n"
  "v_mfma_f32_16x16x32_bf16 v[140:143], v[212:215], v[42:45], v[140:143]\n"
  "v_mfma_f32_16x16x32_bf16 v[128:131], a[0:3], v[64:67], v[128:131]\n"
  "v_mfma_f32_16x16x32_bf16 v[132:135], a[4:7], v[64:67], v[132:135]\n"
  "v_mfma_f32_16x16x32_bf16 v[136:139], a[8:11], v[64:67], v[136:139]\n"
  "v_mfma_f32_16x16x32_bf16 v[140:143], a[12:15], v[64:67], v[140:143]\n"
  "v_mfma_f32_16x16x32_bf16 v[128:131], a[16:19], v[68:71], v[128:131]\n"
  "v_mfma_f32_16x16x32_bf16 v[132:135], a[20:23], v[68:71], v[132:135]\n"
  "v_mfma_f32_16x16x32_bf16 v[136:139], a[24:27], v[68:71], v[136:139]\n"
  "v_mfma_f32_16x16x32_bf16 v[140:143], a[28:31], v[68:71], v[140:143]\n"
  "v_mfma_f32_16x16x32_bf16 v[128:131], a[32:35], v[72:75], v[128:131]\n"
  "v_mfma_f32_16x16x32_bf16 v[132:135], a[36:39], v[72:75], v[132:135]\n"
  "v_mfma_f32_16x16x32_bf16 v[136:139], a[40:43], v[72:75], v[136:139]\n"
  "v_mfma_f32_16x16x32_bf16 v[140:143], a[44:47], v[72:75], v[140:143]\n"
  "v_mfma_f32_16x16x32_bf16 v[128:131], a[48:51], v[76:79], v[128:131]\n"
  "v_mfma_f32_16x16x32_bf16 v[132:135], a[52:55], v[76:79], v[132:135]\n"
  "v_mfma_f32_16x16x32_bf16 v[136:139], a[56:59], v[76:79], v[136:139]\n"
  "v_mfma_f32_16x16x32_bf16 v[140:143], a[60:63], v[76:79], v[140:143]\n"
  "v_mfma_f32_16x16x32_bf16 v[128:131], a[64:67], v[96:99], v[128:131]\n"
  "v_mfma_f32_16x16x32_bf16 v[132:135], a[68:71], v[96:99], v[132:135]\n"
  "v_mfma_f32_16x16x32_bf16 v[136:139], a[72:75], v[96:99], v[136:139]\n"
  "v_mfma_f32_16x16x32_bf16 v[140:143], a[76:79], v[96:99], v[140:143]\n"
  "v_mfma_f32_16x16x32_bf16 v[128:131], a[80:83], v[100:103], v[128:131]\n"
  "v_mfma_f32_16x16x32_bf16 v[132:135], a[84:87], v[100:103], v[132:135]\n"
  "v_mfma_f32_16x16x32_bf16 v[136:139], a[88:91], v[100:103], v[136:139]\n"
  "v_mfma_f32_16x16x32_bf16 v[140:143], a[92:95], v[100:103], v[140:143]\n"
  "v_mfma_f32_16x16x32_bf16 v[128:131], a[96:99], v[104:107], v[128:131]\n"
  "v_mfma_f32_16x16x32_bf16 v[132:135], a[100:103], v[104:107], v[132:135]\n"
  "v_mfma_f32_16x16x32_bf16 v[136:139], a[104:107], v[104:107], v[136:139]\n"
  "v_mfma_f32_16x16x32_bf16 v[140:143], a[108:111], v[104:107], v[140:143]\n"
  "v_mfma_f32_16x16x32_bf16 v[128:131], a[112:115], v[108:111], v[128:131]\n"
  "v_mfma_f32_16x16x32_bf16 v[132:135], a[116:119], v[108:111], v[132:135]\n"
  "v_mfma_f32_16x16x32_bf16 v[136:139], a[120:123], v[108:111], v[136:139]\n"
  "v_mfma_f32_16x16x32_bf16 v[140:143], a[124:127], v[108:111], v[140:143]\n"
  "s_nop 7\n"
  "s_branch L45_%=\n"
  "LEND_%=:\n"
  :: "s"(__builtin_amdgcn_kernarg_segment_ptr()), "s"(blockIdx.x), "v"(threadIdx.x), "s"((unsigned)(size_t)(__attribute__((address_space(3))) char*)smem)
  : "v1","v255","a0","a255","s96","s97","s98","s99","vcc","scc","memory");
}

extern "C" void kernel_launch(void* const* d_in, const int* in_sizes, int n_in, void* d_out, int out_size, void* d_ws, size_t ws_size, hipStream_t stream) {
  Params P;
  P.x = d_in[0]; P.lA = d_in[1]; P.Ai = d_in[2]; P.Cr = d_in[3]; P.Ci = d_in[4]; P.ldt = d_in[5]; P.Dk = d_in[6];
  P.w1 = d_in[8]; P.w3 = d_in[9]; P.w2 = d_in[10]; P.out = d_out; P.ws = d_ws;
  s4d_ffn_kernel<<<256, 256, 0, stream>>>(P);
}
```
